```python
import torch

def _softmax(x, axis=-1):
    return torch.softmax(x, dim=axis)
def _gelu(x):
    return torch.nn.functional.gelu(x, approximate="tanh")
def _silu(x):
    return torch.nn.functional.silu(x)
def _sigmoid(x):
    return torch.sigmoid(x)
def _layernorm(x, axis=-1, eps=1e-5):
    m = x.mean(dim=axis, keepdim=True)
    v = x.var(dim=axis, keepdim=True, unbiased=False)
    return (x - m) / torch.sqrt(v + eps)
def _scatter_add(tgt, idx, src, axis=0):
    return tgt.clone().index_add_(axis, idx, src)
def _segment_sum(src, seg, n):
    out = torch.zeros((n,) + src.shape[1:], dtype=src.dtype)
    return out.index_add_(0, seg, src)
def _pad(x, pw):
    flat = tuple(v for pair in reversed(pw) for v in pair)
    return torch.nn.functional.pad(x, flat)

kernel_name = 'interleave_layers__ssm_conv_h3__glu_gated_swiglu__prompt'

def setup_inputs():

    _w = (lambda a: (lambda t: t.to(torch.bfloat16) if t.is_floating_point() else t)(torch.as_tensor(a)))
    def _A_setup():
        B, S, D = 1, 2048, 576
        ks = 4
        return {
            "q": _w((lambda *s: torch.randn(*s, dtype=torch.bfloat16) * 0.05)(B, S, D)),
            "k": _w((lambda *s: torch.randn(*s, dtype=torch.bfloat16) * 0.2)(B, S, D)),
            "v": _w((lambda *s: torch.randn(*s, dtype=torch.bfloat16) * 0.2)(B, S, D)),
            "shift_k": _w((lambda *s: torch.randn(*s, dtype=torch.bfloat16) * 0.2)(D, ks) * 0.5),
            "ssm_k": _w((lambda *s: torch.randn(*s, dtype=torch.bfloat16) * 0.4)(D, S) * (1.0 / S ** 0.5)),
            "ssm_D": _w((lambda *s: torch.randn(*s, dtype=torch.bfloat16) * 0.2)(D)),
        }
    _A_in = _A_setup()

    def _B_setup():
        B, S, D, F = 1, 2048, 576, 4608
        return {
            "x": _w((lambda *s: torch.randn(*s, dtype=torch.bfloat16) * 0.05)(B, S, D)),
            "w1": _w((lambda *s: torch.randn(*s, dtype=torch.bfloat16) * 0.2)(D, F) * (2.0 / D) ** 0.5),
            "w3": _w((lambda *s: torch.randn(*s, dtype=torch.bfloat16) * 0.2)(D, F) * (2.0 / D) ** 0.5),
            "w2": _w((lambda *s: torch.randn(*s, dtype=torch.bfloat16) * 0.2)(F, D) * (2.0 / F) ** 0.5),
        }
    _B_in = _B_setup()

    return {
        "A_q": _A_in["q"],
        "A_k": _A_in["k"],
        "A_v": _A_in["v"],
        "A_shift_k": _A_in["shift_k"],
        "A_ssm_k": _A_in["ssm_k"],
        "A_ssm_D": _A_in["ssm_D"],
        "B_x": _B_in["x"],
        "B_w1": _B_in["w1"],
        "B_w3": _B_in["w3"],
        "B_w2": _B_in["w2"],
    }

def reference(A_q, A_k, A_v, A_shift_k, A_ssm_k, A_ssm_D, B_x, B_w1, B_w3, B_w2):
    def _A_ref(q, k, v, shift_k, ssm_k, ssm_D):
        B, S, D = q.shape
        ks = shift_k.shape[-1]
        k_pad = _pad(k, ((0, 0), (ks - 1, 0), (0, 0)))
        k_shift = torch.zeros_like(k)
        for t in range(ks):
            k_shift = k_shift + k_pad[:, t:t + S, :] * shift_k[:, ks - 1 - t][None, None, :]
        kv = k_shift * v
        L = 2 * S
        kv_pad = _pad(kv, ((0, 0), (0, S), (0, 0)))
        kf_pad = _pad(ssm_k, ((0, 0), (0, S)))
        kv_f = (lambda x, axes=(-2,-1): torch.fft.rfft2(x.float(), dim=axes))(kv_pad[:, None, :, :], axes=(1, 2))
        kf_f = (lambda x, axes=(-2,-1): torch.fft.rfft2(x.float(), dim=axes))(kf_pad[:, None, :], axes=(1, 2))
        kf_f = (lambda x, *d: x.permute(*d))(kf_f, 1, 2, 0)
        y_f = kv_f * kf_f[None, :, :, :]
        y = (lambda x, s, axes=(-2,-1): torch.fft.irfft2(x, s=s, dim=axes).to(torch.bfloat16))(y_f, (1, L), axes=(1, 2))[:, 0, :S, :]
        y = y + kv * ssm_D[None, None, :]
        return q * y

    def _B_ref(x, w1, w3, w2):
        gate = torch.einsum("bsd,df->bsf", x, w1)
        up = torch.einsum("bsd,df->bsf", x, w3)
        h = _silu(gate) * up
        out = torch.einsum("bsf,fd->bsd", h, w2)
        return out

    x = A_q
    x = x + _A_ref(_layernorm(x, axis=-1), A_k, A_v, A_shift_k, A_ssm_k, A_ssm_D)
    x = x + _B_ref(_layernorm(x, axis=-1), B_w1, B_w3, B_w2)
    x = x + _A_ref(_layernorm(x, axis=-1), A_k, A_v, A_shift_k, A_ssm_k, A_ssm_D)
    x = x + _B_ref(_layernorm(x, axis=-1), B_w1, B_w3, B_w2)
    return x
```

```cpp
#include <hip/hip_runtime.h>
#include <hip/hip_bf16.h>
#include <stdint.h>

typedef __attribute__((ext_vector_type(8))) short bf16x8;
typedef __attribute__((ext_vector_type(4))) float f32x4;
typedef __attribute__((ext_vector_type(16))) float f32x16;
typedef __attribute__((ext_vector_type(4))) unsigned int u32x4;
typedef __attribute__((ext_vector_type(2))) unsigned int u32x2;
typedef __attribute__((ext_vector_type(2))) __bf16 bf16x2_t;
typedef __attribute__((ext_vector_type(2))) float f32x2;
typedef unsigned short u16;

#define AS1 __attribute__((address_space(1)))
#define AS3 __attribute__((address_space(3)))
#define DEVI __device__ __forceinline__

constexpr size_t SZ_W13 = 576ull * 18 * 1024;
constexpr size_t SZ_W2 = 36ull * 144 * 1024;
constexpr size_t SZ_ACT = 2048ull * 576 * 2;
constexpr size_t SZ_H = 128ull * 144 * 1024;
constexpr size_t SZ_P = 4ull * 128 * 36 * 1024;
constexpr size_t OFF_W13 = 0;
constexpr size_t OFF_W2 = OFF_W13 + SZ_W13;
constexpr size_t OFF_Y = OFF_W2 + SZ_W2;
constexpr size_t OFF_X1 = OFF_Y + SZ_ACT;
constexpr size_t OFF_X3 = OFF_X1 + SZ_ACT;
constexpr size_t OFF_XN = OFF_X3 + SZ_ACT;
constexpr size_t OFF_H = OFF_XN + SZ_ACT;
constexpr size_t OFF_P = OFF_H + SZ_H;

DEVI unsigned pack_bf16(float lo, float hi) {
  f32x2 v = {lo, hi};
  bf16x2_t r = __builtin_convertvector(v, bf16x2_t);
  return __builtin_bit_cast(unsigned, r);
}
DEVI float bflo(unsigned u) { return __uint_as_float(u << 16); }
DEVI float bfhi(unsigned u) { return __uint_as_float(u & 0xffff0000u); }
DEVI float rbf(float f) { return bflo(pack_bf16(f, f)); }

template <int N> DEVI void wait_vm() { asm volatile("s_waitcnt vmcnt(%0)" ::"n"(N) : "memory"); }
DEVI void bar() { __builtin_amdgcn_s_barrier(); }

DEVI void glds16(const void* g, void* l) {
  __builtin_amdgcn_global_load_lds((const AS1 void*)g, (AS3 void*)l, 16, 0, 0);
}
DEVI void glds4(const void* g, void* l) {
  __builtin_amdgcn_global_load_lds((const AS1 void*)g, (AS3 void*)l, 4, 0, 0);
}

constexpr int HC_STRIDE = 260 * 16;
constexpr int HC_CH = 8 * HC_STRIDE;
constexpr int UB_CH = 4 * 96 * 16;
constexpr int PREP_LDS = 4 * HC_CH + 4 * UB_CH;

DEVI float chsel(u32x2 r, int ch) { return ch == 0 ? bflo(r.x) : ch == 1 ? bfhi(r.x) : ch == 2 ? bflo(r.y) : bfhi(r.y); }

DEVI void conv_part(const u16* __restrict__ kin, const u16* __restrict__ vin, const u16* __restrict__ shiftk,
                    const u16* __restrict__ ssmk, const u16* __restrict__ ssmD, char* __restrict__ ws, char* smem) {
  const int tid = threadIdx.x, lane = tid & 63;
  const int wave = __builtin_amdgcn_readfirstlane(tid >> 6);
  const int bid = blockIdx.x;
  const int cg = (bid & 7) * 18 + (bid >> 3);
  const int d0 = cg * 4;
  const int s0 = tid * 8;

  u32x2 kr[11], vr[8];
#pragma unroll
  for (int i = 0; i < 11; ++i) {
    int s = s0 - 3 + i;
    u32x2 z = {0u, 0u};
    kr[i] = (s >= 0) ? *(const u32x2*)(kin + (size_t)s * 576 + d0) : z;
  }
#pragma unroll
  for (int i = 0; i < 8; ++i) vr[i] = *(const u32x2*)(vin + (size_t)(s0 + i) * 576 + d0);

  {
    const u16* hsrc = ssmk + (size_t)(d0 + wave) * 2048;
    char* hb = smem + wave * HC_CH;
#pragma unroll
    for (int n = 0; n < 5; ++n) {
      int cp = -4 + lane + 64 * n;
      if (cp <= 254) {
        u32x4 z = {0u, 0u, 0u, 0u};
        u32x4 lo = (cp >= 0) ? *(const u32x4*)(hsrc + 8 * cp) : z;
        u32x4 hi = (cp + 1 >= 0) ? *(const u32x4*)(hsrc + 8 * (cp + 1)) : z;
        unsigned Dw[8] = {lo.x, lo.y, lo.z, lo.w, hi.x, hi.y, hi.z, hi.w};
        unsigned Ow[7];
#pragma unroll
        for (int t = 0; t < 7; ++t) Ow[t] = (Dw[t] >> 16) | (Dw[t + 1] << 16);
        char* dst = hb + (cp + 4) * 16;
#pragma unroll
        for (int p = 0; p < 8; ++p) {
          int pp = p + 1;
          u32x4 o;
          if (pp & 1) {
            int e = (pp - 1) >> 1;
            o = u32x4{Ow[e], Ow[e + 1], Ow[e + 2], Ow[e + 3]};
          } else {
            int e = pp >> 1;
            o = u32x4{Dw[e], Dw[e + 1], Dw[e + 2], Dw[e + 3]};
          }
          *(u32x4*)(dst + p * HC_STRIDE) = o;
        }
      }
    }
  }

  float kv[4][8];
  {
    u32x4 wa = *(const u32x4*)(shiftk + d0 * 4);
    u32x4 wb = *(const u32x4*)(shiftk + d0 * 4 + 8);
    unsigned wq[8] = {wa.x, wa.y, wa.z, wa.w, wb.x, wb.y, wb.z, wb.w};
#pragma unroll
    for (int ch = 0; ch < 4; ++ch) {
      float w[4] = {bflo(wq[2 * ch]), bfhi(wq[2 * ch]), bflo(wq[2 * ch + 1]), bfhi(wq[2 * ch + 1])};
#pragma unroll
      for (int i = 0; i < 8; ++i) {
        float acc = 0.f;
#pragma unroll
        for (int j = 3; j >= 0; --j) acc = rbf(acc + rbf(chsel(kr[i + 3 - j], ch) * w[j]));
        kv[ch][i] = rbf(acc * chsel(vr[i], ch));
      }
    }
  }
  char* ubase = smem + 4 * HC_CH;
  {
    int cidx = 255 - tid;
    int off = ((cidx & 3) * 96 + (cidx >> 2)) * 16;
#pragma unroll
    for (int ch = 0; ch < 4; ++ch) {
      u32x4 o = {pack_bf16(kv[ch][7], kv[ch][6]), pack_bf16(kv[ch][5], kv[ch][4]), pack_bf16(kv[ch][3], kv[ch][2]),
                 pack_bf16(kv[ch][1], kv[ch][0])};
      *(u32x4*)(ubase + ch * UB_CH + off) = o;
    }
#pragma unroll
    for (int r = 0; r < 2; ++r) {
      int idx = tid + 256 * r;
      int ch = idx >> 7, rem = idx & 127;
      u32x4 z = {0u, 0u, 0u, 0u};
      *(u32x4*)(ubase + ch * UB_CH + (((rem >> 5) * 96) + 64 + (rem & 31)) * 16) = z;
    }
  }
  __syncthreads();

  f32x16 acc0 = {}, acc1 = {};
  {
    const int i = lane & 31, hh = lane >> 5;
    const char* pa = smem + wave * HC_CH + (lane & 7) * HC_STRIDE + (hh + (i >> 3)) * 16;
    const char* pb1 = ubase + wave * UB_CH + hh * (96 * 16) + (31 - i) * 16;
    const char* pb0 = pb1 + 32 * 16;
#pragma unroll 4
    for (int q = 0; q < 32; ++q) {
#pragma unroll
      for (int e = 0; e < 2; ++e) {
        bf16x8 a = *(const bf16x8*)(pa + (2 * q + e) * 32);
        bf16x8 b1 = *(const bf16x8*)(pb1 + e * (2 * 96 * 16) + q * 16);
        bf16x8 b0 = *(const bf16x8*)(pb0 + e * (2 * 96 * 16) + q * 16);
        acc1 = __builtin_amdgcn_mfma_f32_32x32x16_bf16(a, b1, acc1, 0, 0, 0);
        acc0 = __builtin_amdgcn_mfma_f32_32x32x16_bf16(a, b0, acc0, 0, 0, 0);
      }
    }
#pragma unroll 4
    for (int q = 32; q < 64; ++q) {
#pragma unroll
      for (int e = 0; e < 2; ++e) {
        bf16x8 a = *(const bf16x8*)(pa + (2 * q + e) * 32);
        bf16x8 b1 = *(const bf16x8*)(pb1 + e * (2 * 96 * 16) + q * 16);
        acc1 = __builtin_amdgcn_mfma_f32_32x32x16_bf16(a, b1, acc1, 0, 0, 0);
      }
    }
  }
  __syncthreads();
  {
    const int i = lane & 31, hh = lane >> 5;
    char* yb = smem + wave * 4096;
#pragma unroll
    for (int q4 = 0; q4 < 4; ++q4) {
      u32x2 o0 = {pack_bf16(acc0[4 * q4], acc0[4 * q4 + 1]), pack_bf16(acc0[4 * q4 + 2], acc0[4 * q4 + 3])};
      u32x2 o1 = {pack_bf16(acc1[4 * q4], acc1[4 * q4 + 1]), pack_bf16(acc1[4 * q4 + 2], acc1[4 * q4 + 3])};
      *(u32x2*)(yb + (32 * i + 8 * q4 + 4 * hh) * 2) = o0;
      *(u32x2*)(yb + (32 * (32 + i) + 8 * q4 + 4 * hh) * 2) = o1;
    }
  }
  __syncthreads();
  {
    u32x2 dd = *(const u32x2*)(ssmD + d0);
    float yv[4][8];
#pragma unroll
    for (int ch = 0; ch < 4; ++ch) {
      u32x4 c = *(const u32x4*)(smem + ch * 4096 + tid * 16);
      float yc[8] = {bflo(c.x), bfhi(c.x), bflo(c.y), bfhi(c.y), bflo(c.z), bfhi(c.z), bflo(c.w), bfhi(c.w)};
      float dv = chsel(dd, ch);
#pragma unroll
      for (int i = 0; i < 8; ++i) yv[ch][i] = rbf(yc[i] + rbf(kv[ch][i] * dv));
    }
    char* yo = ws + OFF_Y + ((size_t)cg * 2048 + s0) * 8;
#pragma unroll
    for (int i = 0; i < 8; i += 2) {
      u32x4 o = {pack_bf16(yv[0][i], yv[1][i]), pack_bf16(yv[2][i], yv[3][i]), pack_bf16(yv[0][i + 1], yv[1][i + 1]),
                 pack_bf16(yv[2][i + 1], yv[3][i + 1])};
      *(u32x4*)(yo + i * 8) = o;
    }
  }
}

DEVI void transpose8(const u32x4 (&r)[8], u32x4 (&o)[8]) {
#pragma unroll
  for (int c = 0; c < 8; ++c) {
    const unsigned sel = (c & 1) ? 0x07060302u : 0x05040100u;
    unsigned d[4];
#pragma unroll
    for (int i = 0; i < 4; ++i) d[i] = __builtin_amdgcn_perm(r[2 * i + 1][c >> 1], r[2 * i][c >> 1], sel);
    o[c] = u32x4{d[0], d[1], d[2], d[3]};
  }
}

DEVI void retile_part(const u16* __restrict__ w1, const u16* __restrict__ w3, const u16* __restrict__ w2,
                      char* __restrict__ ws) {
  const int nthreads = (256 - 144) * 256;
  const int gid = (blockIdx.x - 144) * 256 + threadIdx.x;
  constexpr int NT13 = 2 * 72 * 576;
  constexpr int NT2 = 576 * 72;
  for (int id = gid; id < NT13 + NT2; id += nthreads) {
    u32x4 r[8], o[8];
    if (id < NT13) {
      int fg = id % 576, rest = id / 576;
      int dg = rest % 72, mat = rest / 72;
      const u16* src = (mat ? w3 : w1) + (size_t)(dg * 8) * 4608 + fg * 8;
#pragma unroll
      for (int i = 0; i < 8; ++i) r[i] = *(const u32x4*)(src + (size_t)i * 4608);
      transpose8(r, o);
      int nb = fg / 18, ftl = fg % 18, kt = dg >> 2, G = dg & 3;
      char* dst = ws + OFF_W13 + ((size_t)((nb * 18 + kt) * 18 + ftl) * 64 + G * 16 + mat * 8) * 16;
#pragma unroll
      for (int c = 0; c < 8; ++c) *(u32x4*)(dst + c * 16) = o[c];
    } else {
      int id2 = id - NT13;
      int og = id2 % 72, fg = id2 / 72;
      const u16* src = w2 + (size_t)(fg * 8) * 576 + og * 8;
#pragma unroll
      for (int i = 0; i < 8; ++i) r[i] = *(const u32x4*)(src + (size_t)i * 576);
      transpose8(r, o);
      int kt2 = fg >> 2, G = fg & 3;
      int m = og >> 2, g = og & 3;
#pragma unroll
      for (int e = 0; e < 2; ++e) {
        int ot = 2 * m + e;
        int nb2 = ot / 9, otl = ot % 9;
        char* dst = ws + OFF_W2 + ((size_t)((nb2 * 144 + kt2) * 9 + otl) * 64 + G * 16 + 4 * g) * 16;
#pragma unroll
        for (int c = 0; c < 4; ++c) *(u32x4*)(dst + c * 16) = o[4 * e + c];
      }
    }
  }
}

__global__ __launch_bounds__(256, 1) void k_prep(const u16* __restrict__ kin, const u16* __restrict__ vin,
                                                 const u16* __restrict__ shiftk, const u16* __restrict__ ssmk,
                                                 const u16* __restrict__ ssmD, const u16* __restrict__ w1,
                                                 const u16* __restrict__ w3, const u16* __restrict__ w2,
                                                 char* __restrict__ ws) {
  extern __shared__ __attribute__((aligned(16))) char smem[];
  if (blockIdx.x < 144)
    conv_part(kin, vin, shiftk, ssmk, ssmD, ws, smem);
  else
    retile_part(w1, w3, w2, ws);
}

DEVI float red_tok(float v) {
  v += __shfl_xor(v, 8);
  v += __shfl_xor(v, 16);
  v += __shfl_xor(v, 32);
  return v;
}

DEVI void layernorm8(const float (&x)[8], float (&o)[8], float* red0, float* red1, int wave, int t8) {
  float s = 0.f;
#pragma unroll
  for (int j = 0; j < 8; ++j) s += x[j];
  s = red_tok(s);
  if ((threadIdx.x & 63) < 8) red0[wave * 8 + t8] = s;
  __syncthreads();
  float tot = 0.f;
#pragma unroll
  for (int w = 0; w < 9; ++w) tot += red0[w * 8 + t8];
  float mean = tot * (1.0f / 576.0f);
  float q = 0.f;
#pragma unroll
  for (int j = 0; j < 8; ++j) {
    float d = x[j] - mean;
    q += d * d;
  }
  q = red_tok(q);
  if ((threadIdx.x & 63) < 8) red1[wave * 8 + t8] = q;
  __syncthreads();
  float tq = 0.f;
#pragma unroll
  for (int w = 0; w < 9; ++w) tq += red1[w * 8 + t8];
  float var = rbf(tq * (1.0f / 576.0f));
  float mb = rbf(mean);
  float sd = rbf(sqrtf(rbf(var + 1e-5f)));
#pragma unroll
  for (int j = 0; j < 8; ++j) o[j] = rbf(rbf(x[j] - mb) / sd);
}

template <int MODE>
__global__ __launch_bounds__(576) void k_row(const u16* __restrict__ q, char* __restrict__ ws, u16* __restrict__ out) {
  __shared__ float red[4][72];
  const int tid = threadIdx.x, lane = tid & 63, wave = tid >> 6;
  const int T = blockIdx.x >> 1, half = blockIdx.x & 1;
  const int kp = lane >> 5, G = (lane >> 3) & 3, t8 = lane & 7;
  const int tok = half * 8 + t8, token = T * 16 + tok;
  const int kt = 2 * wave + kp;
  const int chunk = G * 16 + tok;
  const size_t fragoff = ((size_t)(T * 18 + kt) * 64 + chunk) * 16;

  float x[8];
  if (MODE == 0) {
    u32x4 c = *(const u32x4*)(q + (size_t)token * 576 + kt * 32 + G * 8);
    x[0] = bflo(c.x); x[1] = bfhi(c.x); x[2] = bflo(c.y); x[3] = bfhi(c.y);
    x[4] = bflo(c.z); x[5] = bfhi(c.z); x[6] = bflo(c.w); x[7] = bfhi(c.w);
  } else {
    f32x4 pa = {0.f, 0.f, 0.f, 0.f}, pb = {0.f, 0.f, 0.f, 0.f};
#pragma unroll
    for (int sp = 0; sp < 4; ++sp) {
      const char* pp = ws + OFF_P + ((size_t)((sp * 128 + T) * 36 + 2 * kt) * 64 + chunk) * 16;
      pa += *(const f32x4*)pp;
      pb += *(const f32x4*)(pp + 1024);
    }
    u32x4 c = *(const u32x4*)(ws + (MODE == 1 ? OFF_X1 : OFF_X3) + fragoff);
    float xp[8] = {bflo(c.x), bfhi(c.x), bflo(c.y), bfhi(c.y), bflo(c.z), bfhi(c.z), bflo(c.w), bfhi(c.w)};
#pragma unroll
    for (int j = 0; j < 4; ++j) {
      x[j] = rbf(xp[j] + rbf(pa[j]));
      x[4 + j] = rbf(xp[4 + j] + rbf(pb[j]));
    }
  }
  if (MODE == 2) {
    u32x4 o = {pack_bf16(x[0], x[1]), pack_bf16(x[2], x[3]), pack_bf16(x[4], x[5]), pack_bf16(x[6], x[7])};
    *(u32x4*)(out + (size_t)token * 576 + kt * 32 + G * 8) = o;
    return;
  }
  u32x2 ya = *(const u32x2*)(ws + OFF_Y + ((size_t)(8 * kt + 2 * G) * 2048 + token) * 8);
  u32x2 yb = *(const u32x2*)(ws + OFF_Y + ((size_t)(8 * kt + 2 * G + 1) * 2048 + token) * 8);
  float yv[8] = {bflo(ya.x), bfhi(ya.x), bflo(ya.y), bfhi(ya.y), bflo(yb.x), bfhi(yb.x), bflo(yb.y), bfhi(yb.y)};
  float a[8];
  layernorm8(x, a, red[0], red[1], wave, t8);
  float xn[8];
#pragma unroll
  for (int j = 0; j < 8; ++j) xn[j] = rbf(x[j] + rbf(a[j] * yv[j]));
  {
    u32x4 o = {pack_bf16(xn[0], xn[1]), pack_bf16(xn[2], xn[3]), pack_bf16(xn[4], xn[5]), pack_bf16(xn[6], xn[7])};
    *(u32x4*)(ws + (MODE == 0 ? OFF_X1 : OFF_X3) + fragoff) = o;
  }
  float b[8];
  layernorm8(xn, b, red[2], red[3], wave, t8);
  {
    u32x4 o = {pack_bf16(b[0], b[1]), pack_bf16(b[2], b[3]), pack_bf16(b[4], b[5]), pack_bf16(b[6], b[7])};
    size_t off = ((size_t)(((T >> 4) * 18 + kt) * 16 + (T & 15)) * 64 + chunk) * 16;
    *(u32x4*)(ws + OFF_XN + off) = o;
  }
}

constexpr int STAGE_B = 34 * 1024;
constexpr int NSLOT = 4;
constexpr int GEMM_LDS = NSLOT * STAGE_B;

struct G1State {
  f32x4 acc[9][4];
  bf16x8 xf[2][4];
  bf16x8 wf[9];
};

DEVI void g1_issue(const char* px, const char* pw, char* smem, int slot, int tid, int wave) {
  char* l = smem + slot * STAGE_B;
  glds16(px + tid * 16, l + wave * 1024);
  glds16(px + 8192 + tid * 16, l + 8192 + wave * 1024);
  glds16(pw + tid * 16, l + 16384 + wave * 1024);
  glds16(pw + 8192 + tid * 16, l + 24576 + wave * 1024);
  glds4(pw + 16384 + tid * 4, l + 32768 + wave * 256);
}

template <int S>
DEVI void g1_step(G1State& st, const char* px, const char* pw, char* smem, int tid, int wave, const char* lx,
                  const char* lw) {
  constexpr int NS = 18;
  if (S + 1 < NS) {
    wait_vm<(S + 2 < NS) ? 5 : 0>();
    bar();
  }
  if (S + 3 < NS) g1_issue(px + (S + 3) * 16384, pw + (S + 3) * 18432, smem, (S + 3) % NSLOT, tid, wave);
  const char* cw = lw + (S % NSLOT) * STAGE_B;
#pragma unroll
  for (int f = 5; f < 9; ++f) st.wf[f] = *(const bf16x8*)(cw + f * 1024);
#pragma unroll
  for (int f = 0; f < 5; ++f)
#pragma unroll
    for (int t = 0; t < 4; ++t)
      st.acc[f][t] = __builtin_amdgcn_mfma_f32_16x16x32_bf16(st.wf[f], st.xf[S & 1][t], st.acc[f][t], 0, 0, 0);
  if (S + 1 < NS) {
    const char* nx = lx + ((S + 1) % NSLOT) * STAGE_B;
#pragma unroll
    for (int t = 0; t < 4; ++t) st.xf[(S + 1) & 1][t] = *(const bf16x8*)(nx + t * 1024);
  }
#pragma unroll
  for (int f = 5; f < 9; ++f)
#pragma unroll
    for (int t = 0; t < 4; ++t)
      st.acc[f][t] = __builtin_amdgcn_mfma_f32_16x16x32_bf16(st.wf[f], st.xf[S & 1][t], st.acc[f][t], 0, 0, 0);
  if (S + 1 < NS) {
    const char* nw = lw + ((S + 1) % NSLOT) * STAGE_B;
#pragma unroll
    for (int f = 0; f < 5; ++f) st.wf[f] = *(const bf16x8*)(nw + f * 1024);
  }
  if constexpr (S + 1 < NS) g1_step<S + 1>(st, px, pw, smem, tid, wave, lx, lw);
}

DEVI float silu_f(float g) { return g / (1.0f + __expf(-g)); }

__global__ __launch_bounds__(512, 2) void k_gemm1(char* __restrict__ ws) {
  extern __shared__ __attribute__((aligned(16))) char smem[];
  const int tid = threadIdx.x, lane = tid & 63;
  const int wave = __builtin_amdgcn_readfirstlane(tid >> 6);
  const int bid = blockIdx.x, xcd = bid & 7, idx = bid >> 3;
  const int mb = (xcd & 1) * 4 + (idx & 3), nb = (xcd >> 1) * 8 + (idx >> 2);
  const int wm = wave & 3, wn = wave >> 2;
  const char* px = ws + OFF_XN + (size_t)mb * 18 * 16384;
  const char* pw = ws + OFF_W13 + (size_t)nb * 18 * 18432;
  const char* lx = smem + wm * 4096 + lane * 16;
  const char* lw = smem + 16384 + wn * 9216 + lane * 16;

  G1State st;
#pragma unroll
  for (int f = 0; f < 9; ++f)
#pragma unroll
    for (int t = 0; t < 4; ++t) st.acc[f][t] = f32x4{0.f, 0.f, 0.f, 0.f};

  g1_issue(px, pw, smem, 0, tid, wave);
  g1_issue(px + 16384, pw + 18432, smem, 1, tid, wave);
  g1_issue(px + 2 * 16384, pw + 2 * 18432, smem, 2, tid, wave);
  wait_vm<10>();
  bar();
#pragma unroll
  for (int t = 0; t < 4; ++t) st.xf[0][t] = *(const bf16x8*)(lx + t * 1024);
#pragma unroll
  for (int f = 0; f < 5; ++f) st.wf[f] = *(const bf16x8*)(lw + f * 1024);
  g1_step<0>(st, px, pw, smem, tid, wave, lx, lw);

  char* hout = ws + OFF_H;
  const int g2 = (lane >> 4) & 1, tok = lane & 15, th = lane >> 5;
#pragma unroll
  for (int f = 0; f < 9; ++f) {
    const int ftg = nb * 18 + wn * 9 + f;
    const int kt2 = ftg >> 2, G = ftg & 3;
#pragma unroll
    for (int pp = 0; pp < 2; ++pp) {
      float hv[4];
#pragma unroll
      for (int r = 0; r < 4; ++r) {
        auto sw = __builtin_amdgcn_permlane32_swap(__float_as_uint(st.acc[f][2 * pp][r]),
                                                   __float_as_uint(st.acc[f][2 * pp + 1][r]), false, false);
        float g = rbf(__uint_as_float(sw[0]));
        float u = rbf(__uint_as_float(sw[1]));
        hv[r] = rbf(silu_f(g)) * u;
      }
      u32x2 o = {pack_bf16(hv[0], hv[1]), pack_bf16(hv[2], hv[3])};
      const int T = mb * 16 + wm * 4 + 2 * pp + th;
      *(u32x2*)(hout + ((size_t)(T * 144 + kt2) * 64 + G * 16 + tok) * 16 + g2 * 8) = o;
    }
  }
}

struct G2State {
  f32x4 acc[9][2];
};

DEVI void g2_issue(const char* ph0, const char* ph1, const char* pw, char* smem, int slot, int tid, int wave) {
  char* l = smem + slot * STAGE_B;
  glds16(ph0, l + wave * 1024);
  glds16(ph1, l + 8192 + wave * 1024);
  glds16(pw + tid * 16, l + 16384 + wave * 1024);
  glds16(pw + 8192 + tid * 16, l + 24576 + wave * 1024);
  glds4(pw + 16384 + tid * 4, l + 32768 + wave * 256);
}

template <int S>
DEVI void g2_step(G2State& st, const char* ph0, const char* ph1, const char* pw, char* smem, int tid, int wave,
                  const char* lh, const char* lw) {
  constexpr int NS = 18;
  wait_vm<(S + 2 < NS) ? 10 : (S + 1 < NS) ? 5 : 0>();
  bar();
  if (S + 3 < NS) g2_issue(ph0 + (S + 3) * 2048, ph1 + (S + 3) * 2048, pw + (S + 3) * 18432, smem, (S + 3) % NSLOT, tid, wave);
  const char* ch = lh + (S % NSLOT) * STAGE_B;
  const char* cw = lw + (S % NSLOT) * STAGE_B;
  bf16x8 hf[2], wf[9];
#pragma unroll
  for (int t = 0; t < 2; ++t) hf[t] = *(const bf16x8*)(ch + t * 2048);
#pragma unroll
  for (int f = 0; f < 9; ++f) wf[f] = *(const bf16x8*)(cw + f * 1024);
#pragma unroll
  for (int f = 0; f < 9; ++f)
#pragma unroll
    for (int t = 0; t < 2; ++t)
      st.acc[f][t] = __builtin_amdgcn_mfma_f32_16x16x32_bf16(wf[f], hf[t], st.acc[f][t], 0, 0, 0);
  if constexpr (S + 1 < NS) g2_step<S + 1>(st, ph0, ph1, pw, smem, tid, wave, lh, lw);
}

__global__ __launch_bounds__(512, 2) void k_gemm2(char* __restrict__ ws) {
  extern __shared__ __attribute__((aligned(16))) char smem[];
  const int tid = threadIdx.x, lane = tid & 63;
  const int wave = __builtin_amdgcn_readfirstlane(tid >> 6);
  const int bid = blockIdx.x, xcd = bid & 7, idx = bid >> 3;
  const int sp = xcd >> 1, mt = (xcd & 1) * 8 + (idx & 7), nb2 = idx >> 3;
  const int kg = wave >> 2, wt = wave & 3;
  const int fi0 = tid >> 6, fi1 = fi0 + 8;
  const char* hbase = ws + OFF_H;
  const char* ph0 = hbase + ((size_t)((mt * 8 + (fi0 >> 1)) * 144 + sp * 36 + (fi0 & 1)) * 64 + lane) * 16;
  const char* ph1 = hbase + ((size_t)((mt * 8 + (fi1 >> 1)) * 144 + sp * 36 + (fi1 & 1)) * 64 + lane) * 16;
  const char* pw = ws + OFF_W2 + (size_t)(nb2 * 144 + sp * 36) * 9216;
  const char* lh = smem + (wt * 4 + kg) * 1024 + lane * 16;
  const char* lw = smem + 16384 + kg * 9216 + lane * 16;

  G2State st;
#pragma unroll
  for (int f = 0; f < 9; ++f)
#pragma unroll
    for (int t = 0; t < 2; ++t) st.acc[f][t] = f32x4{0.f, 0.f, 0.f, 0.f};

  g2_issue(ph0, ph1, pw, smem, 0, tid, wave);
  g2_issue(ph0 + 2048, ph1 + 2048, pw + 18432, smem, 1, tid, wave);
  g2_issue(ph0 + 4096, ph1 + 4096, pw + 2 * 18432, smem, 2, tid, wave);
  g2_step<0>(st, ph0, ph1, pw, smem, tid, wave, lh, lw);

  __syncthreads();
  char* xb = smem + (wt * 18) * 1024 + lane * 16;
  if (kg == 1) {
#pragma unroll
    for (int f = 0; f < 9; ++f)
#pragma unroll
      for (int t = 0; t < 2; ++t) *(f32x4*)(xb + (f * 2 + t) * 1024) = st.acc[f][t];
  }
  __syncthreads();
  if (kg == 0) {
    char* pout = ws + OFF_P;
#pragma unroll
    for (int f = 0; f < 9; ++f)
#pragma unroll
      for (int t = 0; t < 2; ++t) {
        f32x4 o = st.acc[f][t] + *(const f32x4*)(xb + (f * 2 + t) * 1024);
        const int T = mt * 8 + wt * 2 + t;
        *(f32x4*)(pout + ((size_t)((sp * 128 + T) * 36 + nb2 * 9 + f) * 64 + lane) * 16) = o;
      }
  }
}

extern "C" void kernel_launch(void* const* d_in, const int* in_sizes, int n_in, void* d_out, int out_size, void* d_ws,
                              size_t ws_size, hipStream_t stream) {
  const u16* q = (const u16*)d_in[0];
  const u16* kin = (const u16*)d_in[1];
  const u16* vin = (const u16*)d_in[2];
  const u16* shiftk = (const u16*)d_in[3];
  const u16* ssmk = (const u16*)d_in[4];
  const u16* ssmD = (const u16*)d_in[5];
  const u16* w1 = (const u16*)d_in[7];
  const u16* w3 = (const u16*)d_in[8];
  const u16* w2 = (const u16*)d_in[9];
  char* ws = (char*)d_ws;
  u16* out = (u16*)d_out;

  hipFuncSetAttribute((const void*)k_prep, hipFuncAttributeMaxDynamicSharedMemorySize, PREP_LDS);
  hipFuncSetAttribute((const void*)k_gemm1, hipFuncAttributeMaxDynamicSharedMemorySize, GEMM_LDS);
  hipFuncSetAttribute((const void*)k_gemm2, hipFuncAttributeMaxDynamicSharedMemorySize, GEMM_LDS);

  k_prep<<<256, 256, PREP_LDS, stream>>>(kin, vin, shiftk, ssmk, ssmD, w1, w3, w2, ws);
  k_row<0><<<256, 576, 0, stream>>>(q, ws, out);
  k_gemm1<<<256, 512, GEMM_LDS, stream>>>(ws);
  k_gemm2<<<256, 512, GEMM_LDS, stream>>>(ws);
  k_row<1><<<256, 576, 0, stream>>>(q, ws, out);
  k_gemm1<<<256, 512, GEMM_LDS, stream>>>(ws);
  k_gemm2<<<256, 512, GEMM_LDS, stream>>>(ws);
  k_row<2><<<256, 576, 0, stream>>>(q, ws, out);
}
```

```cpp
#include <hip/hip_runtime.h>
#include <hip/hip_bf16.h>
#include <stdint.h>

typedef __attribute__((ext_vector_type(8))) short bf16x8;
typedef __attribute__((ext_vector_type(4))) float f32x4;
typedef __attribute__((ext_vector_type(16))) float f32x16;
typedef __attribute__((ext_vector_type(4))) unsigned int u32x4;
typedef __attribute__((ext_vector_type(2))) unsigned int u32x2;
typedef __attribute__((ext_vector_type(2))) __bf16 bf16x2_t;
typedef __attribute__((ext_vector_type(2))) float f32x2;
typedef unsigned short u16;

#define AS1 __attribute__((address_space(1)))
#define AS3 __attribute__((address_space(3)))
#define DEVI __device__ __forceinline__

constexpr size_t SZ_W13 = 576ull * 18 * 1024;
constexpr size_t SZ_W2 = 36ull * 144 * 1024;
constexpr size_t SZ_ACT = 2048ull * 576 * 2;
constexpr size_t SZ_H = 128ull * 144 * 1024;
constexpr size_t SZ_P = 4ull * 128 * 36 * 1024;
constexpr size_t OFF_W13 = 0;
constexpr size_t OFF_W2 = OFF_W13 + SZ_W13;
constexpr size_t OFF_Y = OFF_W2 + SZ_W2;
constexpr size_t OFF_X1 = OFF_Y + SZ_ACT;
constexpr size_t OFF_X3 = OFF_X1 + SZ_ACT;
constexpr size_t OFF_XN = OFF_X3 + SZ_ACT;
constexpr size_t OFF_H = OFF_XN + SZ_ACT;
constexpr size_t OFF_P = OFF_H + SZ_H;
constexpr size_t OFF_KVT = OFF_P + SZ_P;

DEVI unsigned pack_bf16(float lo, float hi) {
  f32x2 v = {lo, hi};
  bf16x2_t r = __builtin_convertvector(v, bf16x2_t);
  return __builtin_bit_cast(unsigned, r);
}
DEVI float bflo(unsigned u) { return __uint_as_float(u << 16); }
DEVI float bfhi(unsigned u) { return __uint_as_float(u & 0xffff0000u); }
DEVI float rbf(float f) { return bflo(pack_bf16(f, f)); }

template <int N> DEVI void wait_vm() { asm volatile("s_waitcnt vmcnt(%0)" ::"n"(N) : "memory"); }
DEVI void bar() { __builtin_amdgcn_s_barrier(); }

#define STAMP(kk, id) do {} while (0)

DEVI void glds16(const void* g, void* l) {
  __builtin_amdgcn_global_load_lds((const AS1 void*)g, (AS3 void*)l, 16, 0, 0);
}
DEVI void glds4(const void* g, void* l) {
  __builtin_amdgcn_global_load_lds((const AS1 void*)g, (AS3 void*)l, 4, 0, 0);
}

#define SB() __builtin_amdgcn_sched_barrier(0)
#define WAIT_LGKM0() __builtin_amdgcn_s_waitcnt(0xC07F)
constexpr int HC_STRIDE = 260 * 16;
constexpr int HC_CH = 8 * HC_STRIDE;
constexpr int UB_S = 98;
constexpr int UB_CH = 4 * UB_S * 16;
constexpr int PREP_LDS = 4 * HC_CH + 4 * UB_CH;

DEVI float chsel(u32x2 r, int ch) { return ch == 0 ? bflo(r.x) : ch == 1 ? bfhi(r.x) : ch == 2 ? bflo(r.y) : bfhi(r.y); }

__global__ __launch_bounds__(1024, 4) void k_conv(const u16* __restrict__ ssmk, const u16* __restrict__ ssmD,
                                                 char* __restrict__ ws) {
  extern __shared__ __attribute__((aligned(16))) char smem[];
  const int tid = threadIdx.x, lane = tid & 63;
  const int wave = __builtin_amdgcn_readfirstlane(tid >> 6);
  const int cg = blockIdx.x;
  const int d0 = cg * 4;
  const int s0 = tid * 2;
  char* ubase = smem + 4 * HC_CH;

  STAMP(3, 0);
  {
    const int ch = wave & 3, b8 = wave >> 2;
    glds16(ws + OFF_KVT + (size_t)(d0 + ch) * 4096 + (4 * lane + b8) * 16, ubase + ch * UB_CH + b8 * (UB_S * 16));
  }
  {
    const int ch = wave & 3, qq = wave >> 2;
    const u16* hsrc = ssmk + (size_t)(d0 + ch) * 2048;
    char* hb = smem + ch * HC_CH;
#pragma unroll
    for (int n2 = 0; n2 < 2; ++n2) {
      int cp = -4 + lane + 64 * (n2 == 0 ? qq : 4);
      bool act = (n2 == 0) || (qq == 0 && cp <= 254);
      if (act) {
        int c0 = cp < 0 ? 0 : cp, c1 = cp + 1 < 0 ? 0 : cp + 1;
        u32x4 lo = *(const u32x4*)(hsrc + 8 * c0);
        u32x4 hi = *(const u32x4*)(hsrc + 8 * c1);
        u32x4 z = {0u, 0u, 0u, 0u};
        if (cp < 0) lo = z;
        if (cp + 1 < 0) hi = z;
        unsigned Dw[8] = {lo.x, lo.y, lo.z, lo.w, hi.x, hi.y, hi.z, hi.w};
        unsigned Ow[7];
#pragma unroll
        for (int t = 0; t < 7; ++t) Ow[t] = (Dw[t] >> 16) | (Dw[t + 1] << 16);
        char* dst = hb + (cp + 4) * 16;
#pragma unroll
        for (int p = 0; p < 8; ++p) {
          int pp = p + 1;
          u32x4 o;
          if (pp & 1) {
            int e = (pp - 1) >> 1;
            o = u32x4{Ow[e], Ow[e + 1], Ow[e + 2], Ow[e + 3]};
          } else {
            int e = pp >> 1;
            o = u32x4{Dw[e], Dw[e + 1], Dw[e + 2], Dw[e + 3]};
          }
          *(u32x4*)(dst + p * HC_STRIDE) = o;
        }
      }
    }
  }
  if (tid < 512) {
    int ch = tid >> 7, rem = tid & 127;
    u32x4 z = {0u, 0u, 0u, 0u};
    *(u32x4*)(ubase + ch * UB_CH + (((rem >> 5) * UB_S) + 64 + (rem & 31)) * 16) = z;
  }
  wait_vm<0>();
  __syncthreads();
  STAMP(3, 1);

  if (wave < 4) {
    f32x16 acc0 = {}, acc1 = {};
    const int i = lane & 31, hh = lane >> 5;
    const char* pa = smem + wave * HC_CH + (lane & 7) * HC_STRIDE + (hh + (i >> 3)) * 16;
    const char* pb1 = ubase + wave * UB_CH + hh * (UB_S * 16) + (31 - i) * 16;
    const char* pb0 = pb1 + 32 * 16;
    bf16x8 a[2][2], b1[2][2], b0[2][2];
#define CONV_LD(buf, q, full)                                                    \
  {                                                                              \
    _Pragma("unroll") for (int e = 0; e < 2; ++e) {                              \
      a[buf][e] = *(const bf16x8*)(pa + (2 * (q) + e) * 32);                     \
      b1[buf][e] = *(const bf16x8*)(pb1 + e * (2 * UB_S * 16) + (q) * 16);       \
      if (full) b0[buf][e] = *(const bf16x8*)(pb0 + e * (2 * UB_S * 16) + (q) * 16); \
    }                                                                            \
  }
#define CONV_MM(buf, full)                                                                        \
  {                                                                                               \
    _Pragma("unroll") for (int e = 0; e < 2; ++e) {                                               \
      acc1 = __builtin_amdgcn_mfma_f32_32x32x16_bf16(a[buf][e], b1[buf][e], acc1, 0, 0, 0);       \
      if (full) acc0 = __builtin_amdgcn_mfma_f32_32x32x16_bf16(a[buf][e], b0[buf][e], acc0, 0, 0, 0); \
    }                                                                                             \
  }
    CONV_LD(0, 0, true);
    for (int q = 0; q < 32; q += 2) {
      WAIT_LGKM0(); SB();
      CONV_LD(1, q + 1, true);
      SB();
      CONV_MM(0, true);
      WAIT_LGKM0(); SB();
      if (q + 2 < 32) { CONV_LD(0, q + 2, true); } else { CONV_LD(0, q + 2, false); }
      SB();
      CONV_MM(1, true);
    }
    for (int q = 32; q < 64; q += 2) {
      WAIT_LGKM0(); SB();
      CONV_LD(1, q + 1, false);
      SB();
      CONV_MM(0, false);
      WAIT_LGKM0(); SB();
      if (q + 2 < 64) { CONV_LD(0, q + 2, false); }
      SB();
      CONV_MM(1, false);
    }
    STAMP(3, 2);
    char* yb = smem + wave * HC_CH;
#pragma unroll
    for (int q4 = 0; q4 < 4; ++q4) {
      u32x2 o0 = {pack_bf16(acc0[4 * q4], acc0[4 * q4 + 1]), pack_bf16(acc0[4 * q4 + 2], acc0[4 * q4 + 3])};
      u32x2 o1 = {pack_bf16(acc1[4 * q4], acc1[4 * q4 + 1]), pack_bf16(acc1[4 * q4 + 2], acc1[4 * q4 + 3])};
      *(u32x2*)(yb + (32 * i + 8 * q4 + 4 * hh) * 2) = o0;
      *(u32x2*)(yb + (32 * (32 + i) + 8 * q4 + 4 * hh) * 2) = o1;
    }
  }
  __syncthreads();
  {
    u32x2 dd = *(const u32x2*)(ssmD + d0);
    float yv[4][2];
    int cidx = 255 - (tid >> 2);
    int off = ((cidx & 3) * UB_S + (cidx >> 2)) * 16 + (3 - (tid & 3)) * 4;
#pragma unroll
    for (int ch = 0; ch < 4; ++ch) {
      unsigned c = *(const unsigned*)(smem + ch * HC_CH + tid * 4);
      unsigned kvp = *(const unsigned*)(ubase + ch * UB_CH + off);
      float dv = chsel(dd, ch);
      yv[ch][0] = rbf(bflo(c) + rbf(bfhi(kvp) * dv));
      yv[ch][1] = rbf(bfhi(c) + rbf(bflo(kvp) * dv));
    }
    char* yo = ws + OFF_Y + ((size_t)cg * 2048 + s0) * 8;
    u32x4 o = {pack_bf16(yv[0][0], yv[1][0]), pack_bf16(yv[2][0], yv[3][0]), pack_bf16(yv[0][1], yv[1][1]),
               pack_bf16(yv[2][1], yv[3][1])};
    *(u32x4*)yo = o;
  }
  STAMP(3, 3);
}

DEVI void kvt_part(const u16* __restrict__ kin, const u16* __restrict__ vin, const u16* __restrict__ shiftk,
                   char* __restrict__ ws, int id) {
  const int lane = id & 63, wv = id >> 6;
  const int cgp = wv % 9, tt = wv / 9;
  const int tb = lane >> 3, cc = lane & 7;
  const int s0 = tt * 32 + tb * 4, c0 = cgp * 64 + cc * 8;
  u32x4 kr[7], vr[4], wr[4];
#pragma unroll
  for (int i = 0; i < 7; ++i) {
    int s = s0 - 3 + i;
    s = s < 0 ? 0 : s;
    kr[i] = *(const u32x4*)(kin + (size_t)s * 576 + c0);
  }
#pragma unroll
  for (int i = 0; i < 4; ++i) vr[i] = *(const u32x4*)(vin + (size_t)(s0 + i) * 576 + c0);
#pragma unroll
  for (int i = 0; i < 4; ++i) wr[i] = *(const u32x4*)(shiftk + (size_t)c0 * 4 + i * 8);
  if (s0 == 0) {
    u32x4 z = {0u, 0u, 0u, 0u};
    kr[0] = z; kr[1] = z; kr[2] = z;
  }
#pragma unroll
  for (int ch = 0; ch < 8; ++ch) {
    unsigned w01 = wr[ch >> 1][(ch & 1) * 2], w23 = wr[ch >> 1][(ch & 1) * 2 + 1];
    float w[4] = {bflo(w01), bfhi(w01), bflo(w23), bfhi(w23)};
    float kv[4];
#pragma unroll
    for (int i = 0; i < 4; ++i) {
      float acc = 0.f;
#pragma unroll
      for (int j = 3; j >= 0; --j) {
        unsigned kw = kr[i + 3 - j][ch >> 1];
        acc = rbf(acc + rbf(((ch & 1) ? bfhi(kw) : bflo(kw)) * w[j]));
      }
      unsigned vw = vr[i][ch >> 1];
      kv[i] = acc * ((ch & 1) ? bfhi(vw) : bflo(vw));
    }
    u32x2 o = {pack_bf16(kv[3], kv[2]), pack_bf16(kv[1], kv[0])};
    *(u32x2*)(ws + OFF_KVT + ((size_t)(c0 + ch) * 2048 + 2044 - s0) * 2) = o;
  }
}

DEVI void transpose8(const u32x4 (&r)[8], u32x4 (&o)[8]) {
#pragma unroll
  for (int c = 0; c < 8; ++c) {
    const unsigned sel = (c & 1) ? 0x07060302u : 0x05040100u;
    unsigned d[4];
#pragma unroll
    for (int i = 0; i < 4; ++i) d[i] = __builtin_amdgcn_perm(r[2 * i + 1][c >> 1], r[2 * i][c >> 1], sel);
    o[c] = u32x4{d[0], d[1], d[2], d[3]};
  }
}

DEVI void retile_part(const u16* __restrict__ w1, const u16* __restrict__ w3, const u16* __restrict__ w2,
                      char* __restrict__ ws, int id) {
  constexpr int NT13 = 2 * 72 * 576;
  u32x4 r[8], o[8];
  if (id < NT13) {
    int fg = id % 576, rest = id / 576;
    int dg = rest % 72, mat = rest / 72;
    const u16* src = (mat ? w3 : w1) + (size_t)(dg * 8) * 4608 + fg * 8;
#pragma unroll
    for (int i = 0; i < 8; ++i) r[i] = *(const u32x4*)(src + (size_t)i * 4608);
    transpose8(r, o);
    int nb = fg / 18, ftl = fg % 18, kt = dg >> 2, G = dg & 3;
    char* dst = ws + OFF_W13 + ((size_t)((nb * 18 + kt) * 18 + ftl) * 64 + G * 16 + mat * 2) * 16;
#pragma unroll
    for (int c = 0; c < 8; ++c) *(u32x4*)(dst + (4 * (c >> 1) + (c & 1)) * 16) = o[c];
  } else {
    int id2 = id - NT13;
    int og = id2 % 72, fg = id2 / 72;
    int kt2 = fg >> 2, G = fg & 3;
    const u16* src = w2 + (size_t)(kt2 * 32 + 2 * G) * 576 + og * 8;
#pragma unroll
    for (int i = 0; i < 8; ++i) r[i] = *(const u32x4*)(src + (size_t)(8 * (i >> 1) + (i & 1)) * 576);
    transpose8(r, o);
    int m = og >> 2, g = og & 3;
#pragma unroll
    for (int e = 0; e < 2; ++e) {
      int ot = 2 * m + e;
      int nb2 = ot / 9, otl = ot % 9;
      char* dst = ws + OFF_W2 + ((size_t)((nb2 * 144 + kt2) * 9 + otl) * 64 + G * 16 + 4 * g) * 16;
#pragma unroll
      for (int c = 0; c < 4; ++c) *(u32x4*)(dst + c * 16) = o[4 * e + c];
    }
  }
}

constexpr int NT_KVT = 2048 * 576 / 32;
constexpr int NT_RET = 2 * 72 * 576 + 576 * 72;
__global__ __launch_bounds__(256) void k_pre(const u16* __restrict__ kin, const u16* __restrict__ vin,
                                             const u16* __restrict__ shiftk, const u16* __restrict__ w1,
                                             const u16* __restrict__ w3, const u16* __restrict__ w2,
                                             char* __restrict__ ws) {
  const int id = blockIdx.x * 256 + threadIdx.x;
  if (id < NT_KVT)
    kvt_part(kin, vin, shiftk, ws, id);
  else
    retile_part(w1, w3, w2, ws, id - NT_KVT);
}

DEVI void layernorm8(const float (&x)[8], float (&o)[8], float2* red, int wave, int t8) {
  float m = 0.f;
#pragma unroll
  for (int j = 0; j < 8; ++j) m += x[j];
  m *= 0.125f;
  float M2 = 0.f;
#pragma unroll
  for (int j = 0; j < 8; ++j) {
    float d = x[j] - m;
    M2 += d * d;
  }
  float n2 = 4.f;
#pragma unroll
  for (int sh = 8; sh <= 32; sh <<= 1) {
    float mo = __shfl_xor(m, sh), M2o = __shfl_xor(M2, sh);
    float d = mo - m;
    M2 = M2 + M2o + d * d * n2;
    m = 0.5f * (m + mo);
    n2 *= 2.f;
  }
  if ((threadIdx.x & 63) < 8) red[wave * 8 + t8] = make_float2(m, M2);
  __syncthreads();
  float2 pr[9];
  float mean = 0.f;
#pragma unroll
  for (int w = 0; w < 9; ++w) {
    pr[w] = red[w * 8 + t8];
    mean += pr[w].x;
  }
  mean *= (1.0f / 9.0f);
  float tq = 0.f;
#pragma unroll
  for (int w = 0; w < 9; ++w) {
    float d = pr[w].x - mean;
    tq += pr[w].y + 64.f * d * d;
  }
  float var = rbf(tq * (1.0f / 576.0f));
  float mb = rbf(mean);
  float sd = rbf(__builtin_amdgcn_sqrtf(rbf(var + 1e-5f)));
  float rs = __builtin_amdgcn_rcpf(sd);
#pragma unroll
  for (int j = 0; j < 8; ++j) o[j] = rbf(rbf(x[j] - mb) * rs);
}

template <int MODE>
__global__ __launch_bounds__(576) void k_row(const u16* __restrict__ q, char* __restrict__ ws, u16* __restrict__ out) {
  __shared__ float2 red[2][72];
  const int tid = threadIdx.x, lane = tid & 63, wave = tid >> 6;
  const int T = blockIdx.x >> 1, half = blockIdx.x & 1;
  const int kp = lane >> 5, G = (lane >> 3) & 3, t8 = lane & 7;
  const int tok = half * 8 + t8, token = T * 16 + tok;
  const int kt = 2 * wave + kp;
  const int chunk = G * 16 + tok;
  const size_t fragoff = ((size_t)(T * 18 + kt) * 64 + chunk) * 16;

  float x[8];
  if (MODE == 0) {
    u32x4 c = *(const u32x4*)(q + (size_t)token * 576 + kt * 32 + G * 8);
    x[0] = bflo(c.x); x[1] = bfhi(c.x); x[2] = bflo(c.y); x[3] = bfhi(c.y);
    x[4] = bflo(c.z); x[5] = bfhi(c.z); x[6] = bflo(c.w); x[7] = bfhi(c.w);
  } else {
    f32x4 pa = {0.f, 0.f, 0.f, 0.f}, pb = {0.f, 0.f, 0.f, 0.f};
#pragma unroll
    for (int sp = 0; sp < 4; ++sp) {
      const char* pp = ws + OFF_P + ((size_t)((sp * 128 + T) * 36 + 2 * kt) * 64 + chunk) * 16;
      pa += *(const f32x4*)pp;
      pb += *(const f32x4*)(pp + 1024);
    }
    u32x4 c = *(const u32x4*)(ws + (MODE == 1 ? OFF_X1 : OFF_X3) + fragoff);
    float xp[8] = {bflo(c.x), bfhi(c.x), bflo(c.y), bfhi(c.y), bflo(c.z), bfhi(c.z), bflo(c.w), bfhi(c.w)};
#pragma unroll
    for (int j = 0; j < 4; ++j) {
      x[j] = rbf(xp[j] + rbf(pa[j]));
      x[4 + j] = rbf(xp[4 + j] + rbf(pb[j]));
    }
  }
  if (MODE == 2) {
    u32x4 o = {pack_bf16(x[0], x[1]), pack_bf16(x[2], x[3]), pack_bf16(x[4], x[5]), pack_bf16(x[6], x[7])};
    *(u32x4*)(out + (size_t)token * 576 + kt * 32 + G * 8) = o;
    return;
  }
  u32x2 ya = *(const u32x2*)(ws + OFF_Y + ((size_t)(8 * kt + 2 * G) * 2048 + token) * 8);
  u32x2 yb = *(const u32x2*)(ws + OFF_Y + ((size_t)(8 * kt + 2 * G + 1) * 2048 + token) * 8);
  float yv[8] = {bflo(ya.x), bfhi(ya.x), bflo(ya.y), bfhi(ya.y), bflo(yb.x), bfhi(yb.x), bflo(yb.y), bfhi(yb.y)};
  float a[8];
  layernorm8(x, a, red[0], wave, t8);
  float xn[8];
#pragma unroll
  for (int j = 0; j < 8; ++j) xn[j] = rbf(x[j] + rbf(a[j] * yv[j]));
  {
    u32x4 o = {pack_bf16(xn[0], xn[1]), pack_bf16(xn[2], xn[3]), pack_bf16(xn[4], xn[5]), pack_bf16(xn[6], xn[7])};
    *(u32x4*)(ws + (MODE == 0 ? OFF_X1 : OFF_X3) + fragoff) = o;
  }
  float b[8];
  layernorm8(xn, b, red[1], wave, t8);
  {
    u32x4 o = {pack_bf16(b[0], b[1]), pack_bf16(b[2], b[3]), pack_bf16(b[4], b[5]), pack_bf16(b[6], b[7])};
    size_t off = ((size_t)(((T >> 4) * 18 + kt) * 16 + (T & 15)) * 64 + chunk) * 16;
    *(u32x4*)(ws + OFF_XN + off) = o;
  }
}

constexpr int STAGE_B = 34 * 1024;
constexpr int NSLOT = 4;
constexpr int GEMM_LDS = NSLOT * STAGE_B;

struct G1State {
  f32x4 acc[9][4];
  bf16x8 xf[2][4];
  bf16x8 wf[9];
};

DEVI void g1_issue(const char* px, const char* pw, char* smem, int slot, int tid, int wave) {
  char* l = smem + slot * STAGE_B;
  glds16(px + tid * 16, l + wave * 1024);
  glds16(px + 8192 + tid * 16, l + 8192 + wave * 1024);
  glds16(pw + tid * 16, l + 16384 + wave * 1024);
  glds16(pw + 8192 + tid * 16, l + 24576 + wave * 1024);
  glds4(pw + 16384 + tid * 4, l + 32768 + wave * 256);
}

template <int S>
DEVI void g1_step(G1State& st, const char* px, const char* pw, char* smem, int tid, int wave, const char* lx,
                  const char* lw) {
  constexpr int NS = 18;
  if (S + 1 < NS) {
    wait_vm<(S + 2 < NS) ? 5 : 0>();
    bar();
  }
  WAIT_LGKM0();
  SB();
  const char* cw = lw + (S % NSLOT) * STAGE_B;
#pragma unroll
  for (int f = 5; f < 9; ++f) st.wf[f] = *(const bf16x8*)(cw + f * 1024);
  if (S + 3 < NS) g1_issue(px + (S + 3) * 16384, pw + (S + 3) * 18432, smem, (S + 3) % NSLOT, tid, wave);
  SB();
#pragma unroll
  for (int f = 0; f < 5; ++f)
#pragma unroll
    for (int t = 0; t < 4; ++t)
      st.acc[f][t] = __builtin_amdgcn_mfma_f32_16x16x32_bf16(st.wf[f], st.xf[S & 1][t], st.acc[f][t], 0, 0, 0);
  WAIT_LGKM0();
  SB();
  if (S + 1 < NS) {
    const char* nx = lx + ((S + 1) % NSLOT) * STAGE_B;
#pragma unroll
    for (int t = 0; t < 4; ++t) st.xf[(S + 1) & 1][t] = *(const bf16x8*)(nx + t * 1024);
    const char* nw = lw + ((S + 1) % NSLOT) * STAGE_B;
#pragma unroll
    for (int f = 0; f < 5; ++f) st.wf[f] = *(const bf16x8*)(nw + f * 1024);
  }
  SB();
#pragma unroll
  for (int f = 5; f < 9; ++f)
#pragma unroll
    for (int t = 0; t < 4; ++t)
      st.acc[f][t] = __builtin_amdgcn_mfma_f32_16x16x32_bf16(st.wf[f], st.xf[S & 1][t], st.acc[f][t], 0, 0, 0);
  SB();
  if constexpr (S + 1 < NS) g1_step<S + 1>(st, px, pw, smem, tid, wave, lx, lw);
}

DEVI float silu_f(float g) { return g * __builtin_amdgcn_rcpf(1.0f + __builtin_amdgcn_exp2f(g * -1.44269504088896f)); }

typedef __attribute__((ext_vector_type(3))) unsigned int u32x3;
template <int BM4>
DEVI void g1_epilogue(const G1State& st, char* hb) {
#pragma unroll
  for (int t = 0; t < 4; ++t) {
    unsigned d[9];
#pragma unroll
    for (int f = 0; f < 9; ++f) {
      f32x2 g = {st.acc[f][t][0], st.acc[f][t][1]};
      f32x2 u = {st.acc[f][t][2], st.acc[f][t][3]};
      f32x2 e = g * -1.44269504088896f;
      e[0] = __builtin_amdgcn_exp2f(e[0]);
      e[1] = __builtin_amdgcn_exp2f(e[1]);
      e = e + 1.0f;
      e[0] = __builtin_amdgcn_rcpf(e[0]);
      e[1] = __builtin_amdgcn_rcpf(e[1]);
      f32x2 h = (g * e) * u;
      d[f] = pack_bf16(h[0], h[1]);
    }
    char* p = hb + (size_t)t * 144 * 1024;
    if (BM4 == 0) {
      *(u32x4*)(p) = u32x4{d[0], d[1], d[2], d[3]};
      *(u32x4*)(p + 1024) = u32x4{d[4], d[5], d[6], d[7]};
      *(unsigned*)(p + 2048) = d[8];
    } else if (BM4 == 1) {
      *(u32x3*)(p + 4) = u32x3{d[0], d[1], d[2]};
      *(u32x4*)(p + 1024) = u32x4{d[3], d[4], d[5], d[6]};
      *(u32x2*)(p + 2048) = u32x2{d[7], d[8]};
    } else if (BM4 == 2) {
      *(u32x2*)(p + 8) = u32x2{d[0], d[1]};
      *(u32x4*)(p + 1024) = u32x4{d[2], d[3], d[4], d[5]};
      *(u32x3*)(p + 2048) = u32x3{d[6], d[7], d[8]};
    } else {
      *(unsigned*)(p + 12) = d[0];
      *(u32x4*)(p + 1024) = u32x4{d[1], d[2], d[3], d[4]};
      *(u32x4*)(p + 2048) = u32x4{d[5], d[6], d[7], d[8]};
    }
  }
}

__global__ __launch_bounds__(512, 2) void k_gemm1(char* __restrict__ ws) {
  extern __shared__ __attribute__((aligned(16))) char smem[];
  const int tid = threadIdx.x, lane = tid & 63;
  const int wave = __builtin_amdgcn_readfirstlane(tid >> 6);
  const int bid = blockIdx.x, xcd = bid & 7, idx = bid >> 3;
  const int mb = (xcd & 1) * 4 + (idx & 3), nb = (xcd >> 1) * 8 + (idx >> 2);
  const int wm = wave & 3, wn = wave >> 2;
  const char* px = ws + OFF_XN + (size_t)mb * 18 * 16384;
  const char* pw = ws + OFF_W13 + (size_t)nb * 18 * 18432;
  const char* lx = smem + wm * 4096 + lane * 16;
  const char* lw = smem + 16384 + wn * 9216 + lane * 16;

  STAMP(1, 0);
  G1State st;
#pragma unroll
  for (int f = 0; f < 9; ++f)
#pragma unroll
    for (int t = 0; t < 4; ++t) st.acc[f][t] = f32x4{0.f, 0.f, 0.f, 0.f};

  g1_issue(px, pw, smem, 0, tid, wave);
  g1_issue(px + 16384, pw + 18432, smem, 1, tid, wave);
  g1_issue(px + 2 * 16384, pw + 2 * 18432, smem, 2, tid, wave);
  wait_vm<10>();
  bar();
  STAMP(1, 1);
#pragma unroll
  for (int t = 0; t < 4; ++t) st.xf[0][t] = *(const bf16x8*)(lx + t * 1024);
#pragma unroll
  for (int f = 0; f < 5; ++f) st.wf[f] = *(const bf16x8*)(lw + f * 1024);
  g1_step<0>(st, px, pw, smem, tid, wave, lx, lw);

  STAMP(1, 2);
  char* hout = ws + OFF_H;
  {
    const int base = nb * 18 + wn * 9;
    char* hb = hout + ((size_t)((mb * 16 + wm * 4) * 144 + (base >> 2)) * 64 + lane) * 16;
    switch (base & 3) {
      case 0: g1_epilogue<0>(st, hb); break;
      case 1: g1_epilogue<1>(st, hb); break;
      case 2: g1_epilogue<2>(st, hb); break;
      default: g1_epilogue<3>(st, hb); break;
    }
  }
  STAMP(1, 3);
}

constexpr int STAGE2_B = 18 * 1024;
struct G2State {
  f32x4 acc[9][2];
  bf16x8 hf[4][2];
  bf16x8 wf[2][9];
};

DEVI void gload16(bf16x8& d, const char* p) {
  asm volatile("global_load_dwordx4 %0, %1, off" : "=v"(d) : "v"(p) : "memory");
}

template <int R>
DEVI void g2_issue(G2State& st, const char* ph, const char* pw, char* smem, int tid, int wave) {
  char* l = smem + R * STAGE2_B;
  glds16(pw + tid * 16, l + wave * 1024);
  glds16(pw + 8192 + tid * 16, l + 8192 + wave * 1024);
  glds4(pw + 16384 + tid * 4, l + 16384 + wave * 256);
  gload16(st.hf[R][0], ph);
  gload16(st.hf[R][1], ph + 144 * 1024);
}

template <int S>
DEVI void g2_step(G2State& st, const char* ph, const char* pw, char* smem, int tid, int wave, const char* lw) {
  constexpr int NS = 18;
  if (S + 1 < NS) {
    asm volatile("s_waitcnt vmcnt(%2)" : "+v"(st.hf[(S + 1) & 3][0]), "+v"(st.hf[(S + 1) & 3][1]) : "n"((S + 2 < NS) ? 5 : 0) : "memory");
    bar();
  }
  WAIT_LGKM0();
  SB();
  if (S + 1 < NS) {
    const char* cw = lw + ((S + 1) % NSLOT) * STAGE2_B;
#pragma unroll
    for (int f = 0; f < 9; ++f) st.wf[(S + 1) & 1][f] = *(const bf16x8*)(cw + f * 1024);
  }
  if (S + 3 < NS) g2_issue<(S + 3) & 3>(st, ph + (S + 3) * 2048, pw + (S + 3) * 18432, smem, tid, wave);
  SB();
#pragma unroll
  for (int f = 0; f < 9; ++f)
#pragma unroll
    for (int t = 0; t < 2; ++t)
      st.acc[f][t] = __builtin_amdgcn_mfma_f32_16x16x32_bf16(st.wf[S & 1][f], st.hf[S & 3][t], st.acc[f][t], 0, 0, 0);
  SB();
  if constexpr (S + 1 < NS) g2_step<S + 1>(st, ph, pw, smem, tid, wave, lw);
}

__global__ __launch_bounds__(512, 2) void k_gemm2(char* __restrict__ ws) {
  extern __shared__ __attribute__((aligned(16))) char smem[];
  const int tid = threadIdx.x, lane = tid & 63;
  const int wave = __builtin_amdgcn_readfirstlane(tid >> 6);
  const int bid = blockIdx.x, xcd = bid & 7, idx = bid >> 3;
  const int sp = xcd >> 1, mt = (xcd & 1) * 8 + (idx & 7), nb2 = idx >> 3;
  const int kg = wave >> 2, wt = wave & 3;
  const char* ph = ws + OFF_H + ((size_t)((mt * 8 + 2 * wt) * 144 + sp * 36 + kg) * 64 + lane) * 16;
  const char* pw = ws + OFF_W2 + (size_t)(nb2 * 144 + sp * 36) * 9216;
  const char* lw = smem + kg * 9216 + lane * 16;

  STAMP(2, 0);
  G2State st;
#pragma unroll
  for (int f = 0; f < 9; ++f)
#pragma unroll
    for (int t = 0; t < 2; ++t) st.acc[f][t] = f32x4{0.f, 0.f, 0.f, 0.f};

  g2_issue<0>(st, ph, pw, smem, tid, wave);
  g2_issue<1>(st, ph + 2048, pw + 18432, smem, tid, wave);
  g2_issue<2>(st, ph + 4096, pw + 2 * 18432, smem, tid, wave);
  asm volatile("s_waitcnt vmcnt(10)" : "+v"(st.hf[0][0]), "+v"(st.hf[0][1]) :: "memory");
  bar();
  STAMP(2, 1);
#pragma unroll
  for (int f = 0; f < 9; ++f) st.wf[0][f] = *(const bf16x8*)(lw + f * 1024);
  g2_step<0>(st, ph, pw, smem, tid, wave, lw);
  STAMP(2, 2);
  __syncthreads();
  char* xb = smem + (wt * 18) * 1024 + lane * 16;
  if (kg == 1) {
#pragma unroll
    for (int f = 0; f < 9; ++f)
#pragma unroll
      for (int t = 0; t < 2; ++t) *(f32x4*)(xb + (f * 2 + t) * 1024) = st.acc[f][t];
  }
  __syncthreads();
  if (kg == 0) {
    char* pout = ws + OFF_P;
#pragma unroll
    for (int f = 0; f < 9; ++f)
#pragma unroll
      for (int t = 0; t < 2; ++t) {
        f32x4 o = st.acc[f][t] + *(const f32x4*)(xb + (f * 2 + t) * 1024);
        const int T = mt * 8 + wt * 2 + t;
        *(f32x4*)(pout + ((size_t)((sp * 128 + T) * 36 + nb2 * 9 + f) * 64 + lane) * 16) = o;
      }
  }
  STAMP(2, 3);
}

extern "C" void kernel_launch(void* const* d_in, const int* in_sizes, int n_in, void* d_out, int out_size, void* d_ws,
                              size_t ws_size, hipStream_t stream) {
  const u16* q = (const u16*)d_in[0];
  const u16* kin = (const u16*)d_in[1];
  const u16* vin = (const u16*)d_in[2];
  const u16* shiftk = (const u16*)d_in[3];
  const u16* ssmk = (const u16*)d_in[4];
  const u16* ssmD = (const u16*)d_in[5];
  const u16* w1 = (const u16*)d_in[7];
  const u16* w3 = (const u16*)d_in[8];
  const u16* w2 = (const u16*)d_in[9];
  char* ws = (char*)d_ws;
  u16* out = (u16*)d_out;

  hipFuncSetAttribute((const void*)k_conv, hipFuncAttributeMaxDynamicSharedMemorySize, PREP_LDS);
  hipFuncSetAttribute((const void*)k_gemm1, hipFuncAttributeMaxDynamicSharedMemorySize, GEMM_LDS);
  hipFuncSetAttribute((const void*)k_gemm2, hipFuncAttributeMaxDynamicSharedMemorySize, 4 * STAGE2_B);

  k_pre<<<(NT_KVT + NT_RET) / 256, 256, 0, stream>>>(kin, vin, shiftk, w1, w3, w2, ws);
  k_conv<<<144, 1024, PREP_LDS, stream>>>(ssmk, ssmD, ws);
  k_row<0><<<256, 576, 0, stream>>>(q, ws, out);
  k_gemm1<<<256, 512, GEMM_LDS, stream>>>(ws);
  k_gemm2<<<256, 512, 4 * STAGE2_B, stream>>>(ws);
  k_row<1><<<256, 576, 0, stream>>>(q, ws, out);
  k_gemm1<<<256, 512, GEMM_LDS, stream>>>(ws);
  k_gemm2<<<256, 512, 4 * STAGE2_B, stream>>>(ws);
  k_row<2><<<256, 576, 0, stream>>>(q, ws, out);
}
```

```cpp
#include <hip/hip_runtime.h>
#include <hip/hip_bf16.h>
#include <stdint.h>

typedef __attribute__((ext_vector_type(8))) short bf16x8;
typedef __attribute__((ext_vector_type(4))) float f32x4;
typedef __attribute__((ext_vector_type(16))) float f32x16;
typedef __attribute__((ext_vector_type(4))) unsigned int u32x4;
typedef __attribute__((ext_vector_type(2))) unsigned int u32x2;
typedef __attribute__((ext_vector_type(2))) __bf16 bf16x2_t;
typedef __attribute__((ext_vector_type(2))) float f32x2;
typedef unsigned short u16;

#define AS1 __attribute__((address_space(1)))
#define AS3 __attribute__((address_space(3)))
#define DEVI __device__ __forceinline__

constexpr size_t SZ_W13 = 576ull * 18 * 1024;
constexpr size_t SZ_W2 = 36ull * 144 * 1024;
constexpr size_t SZ_ACT = 2048ull * 576 * 2;
constexpr size_t SZ_H = 128ull * 144 * 1024;
constexpr size_t SZ_P = 4ull * 128 * 36 * 1024;
constexpr size_t OFF_W13 = 0;
constexpr size_t OFF_W2 = OFF_W13 + SZ_W13;
constexpr size_t OFF_Y = OFF_W2 + SZ_W2;
constexpr size_t OFF_X1 = OFF_Y + SZ_ACT;
constexpr size_t OFF_X3 = OFF_X1 + SZ_ACT;
constexpr size_t OFF_XN = OFF_X3 + SZ_ACT;
constexpr size_t OFF_H = OFF_XN + SZ_ACT;
constexpr size_t OFF_P = OFF_H + SZ_H;
constexpr size_t OFF_KVT = OFF_P + SZ_P;
constexpr size_t OFF_CNT = OFF_KVT + SZ_ACT;

DEVI unsigned pack_bf16(float lo, float hi) {
  f32x2 v = {lo, hi};
  bf16x2_t r = __builtin_convertvector(v, bf16x2_t);
  return __builtin_bit_cast(unsigned, r);
}
DEVI float bflo(unsigned u) { return __uint_as_float(u << 16); }
DEVI float bfhi(unsigned u) { return __uint_as_float(u & 0xffff0000u); }
DEVI float rbf(float f) { return bflo(pack_bf16(f, f)); }

template <int N> DEVI void wait_vm() { asm volatile("s_waitcnt vmcnt(%0)" ::"n"(N) : "memory"); }
DEVI void bar() { __builtin_amdgcn_s_barrier(); }

#define STAMP(kk, id) do {} while (0)

DEVI void glds16(const void* g, void* l) {
  __builtin_amdgcn_global_load_lds((const AS1 void*)g, (AS3 void*)l, 16, 0, 0);
}
DEVI void glds4(const void* g, void* l) {
  __builtin_amdgcn_global_load_lds((const AS1 void*)g, (AS3 void*)l, 4, 0, 0);
}

#define SB() __builtin_amdgcn_sched_barrier(0)
#define WAIT_LGKM0() __builtin_amdgcn_s_waitcnt(0xC07F)
constexpr int HC_STRIDE = 260 * 16;
constexpr int HC_CH = 8 * HC_STRIDE;
constexpr int UB_S = 98;
constexpr int UB_CH = 4 * UB_S * 16;
constexpr int PREP_LDS = 4 * HC_CH + 4 * UB_CH;

DEVI float chsel(u32x2 r, int ch) { return ch == 0 ? bflo(r.x) : ch == 1 ? bfhi(r.x) : ch == 2 ? bflo(r.y) : bfhi(r.y); }

__global__ __launch_bounds__(1024, 4) void k_conv(const u16* __restrict__ ssmk, const u16* __restrict__ ssmD,
                                                 char* __restrict__ ws) {
  extern __shared__ __attribute__((aligned(16))) char smem[];
  const int tid = threadIdx.x, lane = tid & 63;
  const int wave = __builtin_amdgcn_readfirstlane(tid >> 6);
  const int cg = blockIdx.x;
  const int d0 = cg * 4;
  const int s0 = tid * 2;
  char* ubase = smem + 4 * HC_CH;

  STAMP(3, 0);
  {
    const int ch = wave & 3, b8 = wave >> 2;
    glds16(ws + OFF_KVT + (size_t)(d0 + ch) * 4096 + (4 * lane + b8) * 16, ubase + ch * UB_CH + b8 * (UB_S * 16));
  }
  {
    const int ch = wave & 3, qq = wave >> 2;
    const u16* hsrc = ssmk + (size_t)(d0 + ch) * 2048;
    char* hb = smem + ch * HC_CH;
#pragma unroll
    for (int n2 = 0; n2 < 2; ++n2) {
      int cp = -4 + lane + 64 * (n2 == 0 ? qq : 4);
      bool act = (n2 == 0) || (qq == 0 && cp <= 254);
      if (act) {
        int c0 = cp < 0 ? 0 : cp, c1 = cp + 1 < 0 ? 0 : cp + 1;
        u32x4 lo = *(const u32x4*)(hsrc + 8 * c0);
        u32x4 hi = *(const u32x4*)(hsrc + 8 * c1);
        u32x4 z = {0u, 0u, 0u, 0u};
        if (cp < 0) lo = z;
        if (cp + 1 < 0) hi = z;
        unsigned Dw[8] = {lo.x, lo.y, lo.z, lo.w, hi.x, hi.y, hi.z, hi.w};
        unsigned Ow[7];
#pragma unroll
        for (int t = 0; t < 7; ++t) Ow[t] = (Dw[t] >> 16) | (Dw[t + 1] << 16);
        char* dst = hb + (cp + 4) * 16;
#pragma unroll
        for (int p = 0; p < 8; ++p) {
          int pp = p + 1;
          u32x4 o;
          if (pp & 1) {
            int e = (pp - 1) >> 1;
            o = u32x4{Ow[e], Ow[e + 1], Ow[e + 2], Ow[e + 3]};
          } else {
            int e = pp >> 1;
            o = u32x4{Dw[e], Dw[e + 1], Dw[e + 2], Dw[e + 3]};
          }
          *(u32x4*)(dst + p * HC_STRIDE) = o;
        }
      }
    }
  }
  if (tid < 512) {
    int ch = tid >> 7, rem = tid & 127;
    u32x4 z = {0u, 0u, 0u, 0u};
    *(u32x4*)(ubase + ch * UB_CH + (((rem >> 5) * UB_S) + 64 + (rem & 31)) * 16) = z;
  }
  wait_vm<0>();
  __syncthreads();
  STAMP(3, 1);

  if (wave < 4) {
    f32x16 acc0 = {}, acc1 = {};
    const int i = lane & 31, hh = lane >> 5;
    const char* pa = smem + wave * HC_CH + (lane & 7) * HC_STRIDE + (hh + (i >> 3)) * 16;
    const char* pb1 = ubase + wave * UB_CH + hh * (UB_S * 16) + (31 - i) * 16;
    const char* pb0 = pb1 + 32 * 16;
    bf16x8 a[2][2], b1[2][2], b0[2][2];
#define CONV_LD(buf, q, full)                                                    \
  {                                                                              \
    _Pragma("unroll") for (int e = 0; e < 2; ++e) {                              \
      a[buf][e] = *(const bf16x8*)(pa + (2 * (q) + e) * 32);                     \
      b1[buf][e] = *(const bf16x8*)(pb1 + e * (2 * UB_S * 16) + (q) * 16);       \
      if (full) b0[buf][e] = *(const bf16x8*)(pb0 + e * (2 * UB_S * 16) + (q) * 16); \
    }                                                                            \
  }
#define CONV_MM(buf, full)                                                                        \
  {                                                                                               \
    _Pragma("unroll") for (int e = 0; e < 2; ++e) {                                               \
      acc1 = __builtin_amdgcn_mfma_f32_32x32x16_bf16(a[buf][e], b1[buf][e], acc1, 0, 0, 0);       \
      if (full) acc0 = __builtin_amdgcn_mfma_f32_32x32x16_bf16(a[buf][e], b0[buf][e], acc0, 0, 0, 0); \
    }                                                                                             \
  }
    CONV_LD(0, 0, true);
    for (int q = 0; q < 32; q += 2) {
      WAIT_LGKM0(); SB();
      CONV_LD(1, q + 1, true);
      SB();
      CONV_MM(0, true);
      WAIT_LGKM0(); SB();
      if (q + 2 < 32) { CONV_LD(0, q + 2, true); } else { CONV_LD(0, q + 2, false); }
      SB();
      CONV_MM(1, true);
    }
    for (int q = 32; q < 64; q += 2) {
      WAIT_LGKM0(); SB();
      CONV_LD(1, q + 1, false);
      SB();
      CONV_MM(0, false);
      WAIT_LGKM0(); SB();
      if (q + 2 < 64) { CONV_LD(0, q + 2, false); }
      SB();
      CONV_MM(1, false);
    }
    STAMP(3, 2);
    char* yb = smem + wave * HC_CH;
#pragma unroll
    for (int q4 = 0; q4 < 4; ++q4) {
      u32x2 o0 = {pack_bf16(acc0[4 * q4], acc0[4 * q4 + 1]), pack_bf16(acc0[4 * q4 + 2], acc0[4 * q4 + 3])};
      u32x2 o1 = {pack_bf16(acc1[4 * q4], acc1[4 * q4 + 1]), pack_bf16(acc1[4 * q4 + 2], acc1[4 * q4 + 3])};
      *(u32x2*)(yb + (32 * i + 8 * q4 + 4 * hh) * 2) = o0;
      *(u32x2*)(yb + (32 * (32 + i) + 8 * q4 + 4 * hh) * 2) = o1;
    }
  }
  __syncthreads();
  {
    u32x2 dd = *(const u32x2*)(ssmD + d0);
    float yv[4][2];
    int cidx = 255 - (tid >> 2);
    int off = ((cidx & 3) * UB_S + (cidx >> 2)) * 16 + (3 - (tid & 3)) * 4;
#pragma unroll
    for (int ch = 0; ch < 4; ++ch) {
      unsigned c = *(const unsigned*)(smem + ch * HC_CH + tid * 4);
      unsigned kvp = *(const unsigned*)(ubase + ch * UB_CH + off);
      float dv = chsel(dd, ch);
      yv[ch][0] = rbf(bflo(c) + rbf(bfhi(kvp) * dv));
      yv[ch][1] = rbf(bfhi(c) + rbf(bflo(kvp) * dv));
    }
    char* yo = ws + OFF_Y + ((size_t)cg * 2048 + s0) * 8;
    u32x4 o = {pack_bf16(yv[0][0], yv[1][0]), pack_bf16(yv[2][0], yv[3][0]), pack_bf16(yv[0][1], yv[1][1]),
               pack_bf16(yv[2][1], yv[3][1])};
    *(u32x4*)yo = o;
  }
  STAMP(3, 3);
}

DEVI void kvt_part(const u16* __restrict__ kin, const u16* __restrict__ vin, const u16* __restrict__ shiftk,
                   char* __restrict__ ws, int id) {
  const int lane = id & 63, wv = id >> 6;
  const int cgp = wv % 9, tt = wv / 9;
  const int tb = lane >> 3, cc = lane & 7;
  const int s0 = tt * 32 + tb * 4, c0 = cgp * 64 + cc * 8;
  u32x4 kr[7], vr[4], wr[4];
#pragma unroll
  for (int i = 0; i < 7; ++i) {
    int s = s0 - 3 + i;
    s = s < 0 ? 0 : s;
    kr[i] = *(const u32x4*)(kin + (size_t)s * 576 + c0);
  }
#pragma unroll
  for (int i = 0; i < 4; ++i) vr[i] = *(const u32x4*)(vin + (size_t)(s0 + i) * 576 + c0);
#pragma unroll
  for (int i = 0; i < 4; ++i) wr[i] = *(const u32x4*)(shiftk + (size_t)c0 * 4 + i * 8);
  if (s0 == 0) {
    u32x4 z = {0u, 0u, 0u, 0u};
    kr[0] = z; kr[1] = z; kr[2] = z;
  }
#pragma unroll
  for (int ch = 0; ch < 8; ++ch) {
    unsigned w01 = wr[ch >> 1][(ch & 1) * 2], w23 = wr[ch >> 1][(ch & 1) * 2 + 1];
    float w[4] = {bflo(w01), bfhi(w01), bflo(w23), bfhi(w23)};
    float kv[4];
#pragma unroll
    for (int i = 0; i < 4; ++i) {
      float acc = 0.f;
#pragma unroll
      for (int j = 3; j >= 0; --j) {
        unsigned kw = kr[i + 3 - j][ch >> 1];
        acc = rbf(acc + rbf(((ch & 1) ? bfhi(kw) : bflo(kw)) * w[j]));
      }
      unsigned vw = vr[i][ch >> 1];
      kv[i] = acc * ((ch & 1) ? bfhi(vw) : bflo(vw));
    }
    u32x2 o = {pack_bf16(kv[3], kv[2]), pack_bf16(kv[1], kv[0])};
    *(u32x2*)(ws + OFF_KVT + ((size_t)(c0 + ch) * 2048 + 2044 - s0) * 2) = o;
  }
}

DEVI void transpose8(const u32x4 (&r)[8], u32x4 (&o)[8]) {
#pragma unroll
  for (int c = 0; c < 8; ++c) {
    const unsigned sel = (c & 1) ? 0x07060302u : 0x05040100u;
    unsigned d[4];
#pragma unroll
    for (int i = 0; i < 4; ++i) d[i] = __builtin_amdgcn_perm(r[2 * i + 1][c >> 1], r[2 * i][c >> 1], sel);
    o[c] = u32x4{d[0], d[1], d[2], d[3]};
  }
}

DEVI void retile_part(const u16* __restrict__ w1, const u16* __restrict__ w3, const u16* __restrict__ w2,
                      char* __restrict__ ws, int id) {
  constexpr int NT13 = 2 * 72 * 576;
  u32x4 r[8], o[8];
  if (id < NT13) {
    int fg = id % 576, rest = id / 576;
    int dg = rest % 72, mat = rest / 72;
    const u16* src = (mat ? w3 : w1) + (size_t)(dg * 8) * 4608 + fg * 8;
#pragma unroll
    for (int i = 0; i < 8; ++i) r[i] = *(const u32x4*)(src + (size_t)i * 4608);
    transpose8(r, o);
    int nb = fg / 18, ftl = fg % 18, kt = dg >> 2, G = dg & 3;
    char* dst = ws + OFF_W13 + ((size_t)((nb * 18 + kt) * 18 + ftl) * 64 + G * 16 + mat * 2) * 16;
#pragma unroll
    for (int c = 0; c < 8; ++c) *(u32x4*)(dst + (4 * (c >> 1) + (c & 1)) * 16) = o[c];
  } else {
    int id2 = id - NT13;
    int og = id2 % 72, fg = id2 / 72;
    int kt2 = fg >> 2, G = fg & 3;
    const u16* src = w2 + (size_t)(kt2 * 32 + 2 * G) * 576 + og * 8;
#pragma unroll
    for (int i = 0; i < 8; ++i) r[i] = *(const u32x4*)(src + (size_t)(8 * (i >> 1) + (i & 1)) * 576);
    transpose8(r, o);
    int m = og >> 2, g = og & 3;
#pragma unroll
    for (int e = 0; e < 2; ++e) {
      int ot = 2 * m + e;
      int nb2 = ot / 9, otl = ot % 9;
      char* dst = ws + OFF_W2 + ((size_t)((nb2 * 144 + kt2) * 9 + otl) * 64 + G * 16 + 4 * g) * 16;
#pragma unroll
      for (int c = 0; c < 4; ++c) *(u32x4*)(dst + c * 16) = o[4 * e + c];
    }
  }
}

constexpr int NT_KVT = 2048 * 576 / 32;
constexpr int NT_RET = 2 * 72 * 576 + 576 * 72;
__global__ __launch_bounds__(256) void k_pre(const u16* __restrict__ kin, const u16* __restrict__ vin,
                                             const u16* __restrict__ shiftk, const u16* __restrict__ w1,
                                             const u16* __restrict__ w3, const u16* __restrict__ w2,
                                             char* __restrict__ ws) {
  const int id = blockIdx.x * 256 + threadIdx.x;
  if (id < 128) *(unsigned*)(ws + OFF_CNT + (size_t)id * 256) = 0u;
  if (id < NT_KVT)
    kvt_part(kin, vin, shiftk, ws, id);
  else
    retile_part(w1, w3, w2, ws, id - NT_KVT);
}

constexpr int C_XN = 0;
constexpr int C_H = 16;
constexpr int C_P = 80;
constexpr int N_CNT = 112;

typedef __attribute__((ext_vector_type(3))) unsigned int u32x3;
#define SC1 16
DEVI void st16(__amdgpu_buffer_rsrc_t r, unsigned off, u32x4 v) { __builtin_amdgcn_raw_buffer_store_b128(v, r, off, 0, SC1); }
DEVI void st12(__amdgpu_buffer_rsrc_t r, unsigned off, u32x3 v) { __builtin_amdgcn_raw_buffer_store_b96(v, r, off, 0, SC1); }
DEVI void st8(__amdgpu_buffer_rsrc_t r, unsigned off, u32x2 v) { __builtin_amdgcn_raw_buffer_store_b64(v, r, off, 0, SC1); }
DEVI void st4(__amdgpu_buffer_rsrc_t r, unsigned off, unsigned v) { __builtin_amdgcn_raw_buffer_store_b32(v, r, off, 0, SC1); }

DEVI unsigned* cnt_ptr(char* ws, int idx) { return (unsigned*)(ws + OFF_CNT + (size_t)idx * 256); }
DEVI void signal_cnt(char* ws, int idx) {
  asm volatile("s_waitcnt vmcnt(0)" ::: "memory");
  __syncthreads();
  if (threadIdx.x == 0) __hip_atomic_fetch_add(cnt_ptr(ws, idx), 1u, __ATOMIC_RELAXED, __HIP_MEMORY_SCOPE_AGENT);
}
DEVI void wait_cnt(char* ws, int idx, unsigned target) {
  if (threadIdx.x == 0) {
    unsigned* c = cnt_ptr(ws, idx);
    unsigned spins = 0;
    while (__hip_atomic_load(c, __ATOMIC_RELAXED, __HIP_MEMORY_SCOPE_AGENT) < target) {
      __builtin_amdgcn_s_sleep(1);
      if (++spins > (1u << 18)) break;
    }
    __builtin_amdgcn_fence(__ATOMIC_ACQUIRE, "agent");
    asm volatile("s_waitcnt vmcnt(0)" ::: "memory");
  }
  __syncthreads();
}

struct LNStat { float m, M2; };
DEVI LNStat ln_partial(const float (&x)[8]) {
  float m = 0.f;
#pragma unroll
  for (int j = 0; j < 8; ++j) m += x[j];
  m *= 0.125f;
  float M2 = 0.f;
#pragma unroll
  for (int j = 0; j < 8; ++j) {
    float d = x[j] - m;
    M2 += d * d;
  }
  float n2 = 4.f;
#pragma unroll
  for (int sh = 8; sh <= 32; sh <<= 1) {
    float mo = __shfl_xor(m, sh), M2o = __shfl_xor(M2, sh);
    float d = mo - m;
    M2 = M2 + M2o + d * d * n2;
    m = 0.5f * (m + mo);
    n2 *= 2.f;
  }
  return LNStat{m, M2};
}
DEVI void ln_final(const float2* red, int t8, float& mb, float& rs) {
  float2 pr[9];
  float mean = 0.f;
#pragma unroll
  for (int w = 0; w < 9; ++w) {
    pr[w] = red[w * 8 + t8];
    mean += pr[w].x;
  }
  mean *= (1.0f / 9.0f);
  float tq = 0.f;
#pragma unroll
  for (int w = 0; w < 9; ++w) {
    float d = pr[w].x - mean;
    tq += pr[w].y + 64.f * d * d;
  }
  float var = rbf(tq * (1.0f / 576.0f));
  mb = rbf(mean);
  float sd = rbf(__builtin_amdgcn_sqrtf(rbf(var + 1e-5f)));
  rs = __builtin_amdgcn_rcpf(sd);
}
DEVI void unpack8(u32x4 c, float (&x)[8]) {
  x[0] = bflo(c.x); x[1] = bfhi(c.x); x[2] = bflo(c.y); x[3] = bfhi(c.y);
  x[4] = bflo(c.z); x[5] = bfhi(c.z); x[6] = bflo(c.w); x[7] = bfhi(c.w);
}
DEVI u32x4 pack8(const float (&x)[8]) {
  return u32x4{pack_bf16(x[0], x[1]), pack_bf16(x[2], x[3]), pack_bf16(x[4], x[5]), pack_bf16(x[6], x[7])};
}

template <int MODE, int NV>
DEVI void row_body(const u16* __restrict__ q, char* __restrict__ ws, __amdgpu_buffer_rsrc_t rs_ws, u16* __restrict__ out,
                   char* smem, int T, int half, int wave, int lane) {
  float2* red = (float2*)smem;
  const int kp = lane >> 5, G = (lane >> 3) & 3, t8 = lane & 7;
  const int tok = half * 8 + t8, token = T * 16 + tok;
  const int chunk = G * 16 + tok;
  float x[NV][8], yv[NV][8];
#pragma unroll
  for (int v = 0; v < NV; ++v) {
    const int vw = v == 0 ? wave : 8;
    const int kt = 2 * vw + kp;
    if (MODE == 0) {
      unpack8(*(const u32x4*)(q + (size_t)token * 576 + kt * 32 + G * 8), x[v]);
    } else {
      f32x4 pa = {0.f, 0.f, 0.f, 0.f}, pb = {0.f, 0.f, 0.f, 0.f};
#pragma unroll
      for (int sp = 0; sp < 4; ++sp) {
        const char* pp = ws + OFF_P + ((size_t)((sp * 128 + T) * 36 + 2 * kt) * 64 + chunk) * 16;
        pa += *(const f32x4*)pp;
        pb += *(const f32x4*)(pp + 1024);
      }
      float xp[8];
      unpack8(*(const u32x4*)(ws + (MODE == 1 ? OFF_X1 : OFF_X3) + ((size_t)(T * 18 + kt) * 64 + chunk) * 16), xp);
#pragma unroll
      for (int j = 0; j < 4; ++j) {
        x[v][j] = rbf(xp[j] + rbf(pa[j]));
        x[v][4 + j] = rbf(xp[4 + j] + rbf(pb[j]));
      }
    }
    if (MODE == 2) {
      *(u32x4*)(out + (size_t)token * 576 + kt * 32 + G * 8) = pack8(x[v]);
    } else {
      u32x2 ya = *(const u32x2*)(ws + OFF_Y + ((size_t)(8 * kt + 2 * G) * 2048 + token) * 8);
      u32x2 yb = *(const u32x2*)(ws + OFF_Y + ((size_t)(8 * kt + 2 * G + 1) * 2048 + token) * 8);
      unpack8(u32x4{ya.x, ya.y, yb.x, yb.y}, yv[v]);
    }
  }
  if (MODE == 2) return;
#pragma unroll
  for (int v = 0; v < NV; ++v) {
    LNStat st = ln_partial(x[v]);
    if (lane < 8) red[(v == 0 ? wave : 8) * 8 + t8] = make_float2(st.m, st.M2);
  }
  __syncthreads();
  float mb, rs;
  ln_final(red, t8, mb, rs);
#pragma unroll
  for (int v = 0; v < NV; ++v) {
    const int kt = 2 * (v == 0 ? wave : 8) + kp;
#pragma unroll
    for (int j = 0; j < 8; ++j) x[v][j] = rbf(x[v][j] + rbf(rbf(rbf(x[v][j] - mb) * rs) * yv[v][j]));
    *(u32x4*)(ws + (MODE == 0 ? OFF_X1 : OFF_X3) + ((size_t)(T * 18 + kt) * 64 + chunk) * 16) = pack8(x[v]);
    LNStat st = ln_partial(x[v]);
    if (lane < 8) red[72 + (v == 0 ? wave : 8) * 8 + t8] = make_float2(st.m, st.M2);
  }
  __syncthreads();
  ln_final(red + 72, t8, mb, rs);
#pragma unroll
  for (int v = 0; v < NV; ++v) {
    const int kt = 2 * (v == 0 ? wave : 8) + kp;
    float b[8];
#pragma unroll
    for (int j = 0; j < 8; ++j) b[j] = rbf(rbf(x[v][j] - mb) * rs);
    unsigned off = (unsigned)(OFF_XN + ((size_t)(((T >> 4) * 18 + kt) * 16 + (T & 15)) * 64 + chunk) * 16);
    st16(rs_ws, off, pack8(b));
  }
}

template <int MODE>
DEVI void row_phase(const u16* __restrict__ q, char* __restrict__ ws, __amdgpu_buffer_rsrc_t rs_ws, u16* __restrict__ out,
                    char* smem, int T, int half) {
  int tid = threadIdx.x; asm volatile("" : "+v"(tid)); const int lane = tid & 63; const int wave = __builtin_amdgcn_readfirstlane(tid >> 6);
  if (wave == 0)
    row_body<MODE, 2>(q, ws, rs_ws, out, smem, T, half, wave, lane);
  else
    row_body<MODE, 1>(q, ws, rs_ws, out, smem, T, half, wave, lane);
}

constexpr int STAGE_B = 34 * 1024;
constexpr int NSLOT = 4;
constexpr int GEMM_LDS = NSLOT * STAGE_B;
struct G1State {
  f32x4 acc[9][4];
  bf16x8 xf[2][4];
  bf16x8 wf[9];
};
DEVI void g1_issue_x(const char* px, char* smem, int slot, int tid, int wave) {
  char* l = smem + slot * STAGE_B;
  glds16(px + tid * 16, l + wave * 1024);
  glds16(px + 8192 + tid * 16, l + 8192 + wave * 1024);
}
DEVI void g1_issue_w(const char* pw, char* smem, int slot, int tid, int wave) {
  char* l = smem + slot * STAGE_B;
  glds16(pw + tid * 16, l + 16384 + wave * 1024);
  glds16(pw + 8192 + tid * 16, l + 24576 + wave * 1024);
  glds4(pw + 16384 + tid * 4, l + 32768 + wave * 256);
}

template <int S>
DEVI void g1_step(G1State& st, const char* px, const char* pw, char* smem, int tid, int wave, const char* lx,
                  const char* lw) {
  constexpr int NS = 18;
  if (S + 1 < NS) {
    wait_vm<(S == 0) ? 2 : (S + 2 < NS) ? 5 : 0>();
    bar();
  }
  WAIT_LGKM0();
  SB();
  const char* cw = lw + (S % NSLOT) * STAGE_B;
#pragma unroll
  for (int f = 5; f < 9; ++f) st.wf[f] = *(const bf16x8*)(cw + f * 1024);
  if (S + 3 < NS) {
    g1_issue_x(px + (S + 3) * 16384, smem, (S + 3) % NSLOT, tid, wave);
    g1_issue_w(pw + (S + 3) * 18432, smem, (S + 3) % NSLOT, tid, wave);
  }
  SB();
#pragma unroll
  for (int f = 0; f < 5; ++f)
#pragma unroll
    for (int t = 0; t < 4; ++t)
      st.acc[f][t] = __builtin_amdgcn_mfma_f32_16x16x32_bf16(st.wf[f], st.xf[S & 1][t], st.acc[f][t], 0, 0, 0);
  WAIT_LGKM0();
  SB();
  if (S + 1 < NS) {
    const char* nx = lx + ((S + 1) % NSLOT) * STAGE_B;
#pragma unroll
    for (int t = 0; t < 4; ++t) st.xf[(S + 1) & 1][t] = *(const bf16x8*)(nx + t * 1024);
    const char* nw = lw + ((S + 1) % NSLOT) * STAGE_B;
#pragma unroll
    for (int f = 0; f < 5; ++f) st.wf[f] = *(const bf16x8*)(nw + f * 1024);
  }
  SB();
#pragma unroll
  for (int f = 5; f < 9; ++f)
#pragma unroll
    for (int t = 0; t < 4; ++t)
      st.acc[f][t] = __builtin_amdgcn_mfma_f32_16x16x32_bf16(st.wf[f], st.xf[S & 1][t], st.acc[f][t], 0, 0, 0);
  SB();
  if constexpr (S + 1 < NS) g1_step<S + 1>(st, px, pw, smem, tid, wave, lx, lw);
}

template <int BM4>
DEVI void g1_epilogue(const G1State& st, __amdgpu_buffer_rsrc_t r, unsigned hb) {
#pragma unroll
  for (int t = 0; t < 4; ++t) {
    unsigned d[9];
#pragma unroll
    for (int f = 0; f < 9; ++f) {
      f32x2 g = {st.acc[f][t][0], st.acc[f][t][1]};
      f32x2 u = {st.acc[f][t][2], st.acc[f][t][3]};
      f32x2 e = g * -1.44269504088896f;
      e[0] = __builtin_amdgcn_exp2f(e[0]);
      e[1] = __builtin_amdgcn_exp2f(e[1]);
      e = e + 1.0f;
      e[0] = __builtin_amdgcn_rcpf(e[0]);
      e[1] = __builtin_amdgcn_rcpf(e[1]);
      f32x2 h = (g * e) * u;
      d[f] = pack_bf16(h[0], h[1]);
    }
    unsigned p = hb + (unsigned)t * 144 * 1024;
    if (BM4 == 0) {
      st16(r, p, u32x4{d[0], d[1], d[2], d[3]});
      st16(r, p + 1024, u32x4{d[4], d[5], d[6], d[7]});
      st4(r, p + 2048, d[8]);
    } else if (BM4 == 1) {
      st12(r, p + 4, u32x3{d[0], d[1], d[2]});
      st16(r, p + 1024, u32x4{d[3], d[4], d[5], d[6]});
      st8(r, p + 2048, u32x2{d[7], d[8]});
    } else if (BM4 == 2) {
      st8(r, p + 8, u32x2{d[0], d[1]});
      st16(r, p + 1024, u32x4{d[2], d[3], d[4], d[5]});
      st12(r, p + 2048, u32x3{d[6], d[7], d[8]});
    } else {
      st4(r, p + 12, d[0]);
      st16(r, p + 1024, u32x4{d[1], d[2], d[3], d[4]});
      st16(r, p + 2048, u32x4{d[5], d[6], d[7], d[8]});
    }
  }
}

#define THREAD_IDS() int tid = threadIdx.x; asm volatile("" : "+v"(tid)); const int lane = tid & 63; const int wave = __builtin_amdgcn_readfirstlane(tid >> 6)
DEVI void g1_phase(char* __restrict__ ws, __amdgpu_buffer_rsrc_t rs_ws, char* smem, int L, int mb, int nb) {
  THREAD_IDS();
  const int wm = wave & 3, wn = wave >> 2;
  const char* px = ws + OFF_XN + (size_t)mb * 18 * 16384;
  const char* pw = ws + OFF_W13 + (size_t)nb * 18 * 18432;
  const char* lx = smem + wm * 4096 + lane * 16;
  const char* lw = smem + 16384 + wn * 9216 + lane * 16;
  g1_issue_w(pw, smem, 0, tid, wave);
  g1_issue_w(pw + 18432, smem, 1, tid, wave);
  g1_issue_w(pw + 2 * 18432, smem, 2, tid, wave);
  wait_cnt(ws, C_XN + L * 8 + mb, 32);
  g1_issue_x(px, smem, 0, tid, wave);
  g1_issue_x(px + 16384, smem, 1, tid, wave);
  g1_issue_x(px + 2 * 16384, smem, 2, tid, wave);
  G1State st;
#pragma unroll
  for (int f = 0; f < 9; ++f)
#pragma unroll
    for (int t = 0; t < 4; ++t) st.acc[f][t] = f32x4{0.f, 0.f, 0.f, 0.f};
  wait_vm<4>();
  bar();
#pragma unroll
  for (int t = 0; t < 4; ++t) st.xf[0][t] = *(const bf16x8*)(lx + t * 1024);
#pragma unroll
  for (int f = 0; f < 5; ++f) st.wf[f] = *(const bf16x8*)(lw + f * 1024);
  g1_step<0>(st, px, pw, smem, tid, wave, lx, lw);
  {
    const int base = nb * 18 + wn * 9;
    unsigned hb = (unsigned)(OFF_H + ((size_t)((mb * 16 + wm * 4) * 144 + (base >> 2)) * 64 + lane) * 16);
    switch (base & 3) {
      case 0: g1_epilogue<0>(st, rs_ws, hb); break;
      case 1: g1_epilogue<1>(st, rs_ws, hb); break;
      case 2: g1_epilogue<2>(st, rs_ws, hb); break;
      default: g1_epilogue<3>(st, rs_ws, hb); break;
    }
  }
  signal_cnt(ws, C_H + L * 32 + mb * 4 + (nb >> 3));
}

constexpr int STAGE2_B = 18 * 1024;
struct G2State {
  f32x4 acc[9][2];
  bf16x8 hf[4][2];
  bf16x8 wf[2][9];
};
DEVI void gload16(bf16x8& d, const char* p) {
  asm volatile("global_load_dwordx4 %0, %1, off" : "=v"(d) : "v"(p) : "memory");
}
template <int R>
DEVI void g2_issue_w(const char* pw, char* smem, int tid, int wave) {
  char* l = smem + R * STAGE2_B;
  glds16(pw + tid * 16, l + wave * 1024);
  glds16(pw + 8192 + tid * 16, l + 8192 + wave * 1024);
  glds4(pw + 16384 + tid * 4, l + 16384 + wave * 256);
}
template <int R>
DEVI void g2_issue_h(G2State& st, const char* ph) {
  gload16(st.hf[R][0], ph);
  gload16(st.hf[R][1], ph + 144 * 1024);
}

template <int S>
DEVI void g2_step(G2State& st, const char* ph, const char* pw, char* smem, int tid, int wave, const char* lw) {
  constexpr int NS = 18;
  if (S + 1 < NS) {
    asm volatile("s_waitcnt vmcnt(%2)" : "+v"(st.hf[(S + 1) & 3][0]), "+v"(st.hf[(S + 1) & 3][1]) : "n"((S == 0) ? 2 : (S + 2 < NS) ? 5 : 0) : "memory");
    bar();
  }
  WAIT_LGKM0();
  SB();
  if (S + 1 < NS) {
    const char* cw = lw + ((S + 1) % NSLOT) * STAGE2_B;
#pragma unroll
    for (int f = 0; f < 9; ++f) st.wf[(S + 1) & 1][f] = *(const bf16x8*)(cw + f * 1024);
  }
  if (S + 3 < NS) {
    g2_issue_w<(S + 3) & 3>(pw + (S + 3) * 18432, smem, tid, wave);
    g2_issue_h<(S + 3) & 3>(st, ph + (S + 3) * 2048);
  }
  SB();
#pragma unroll
  for (int f = 0; f < 9; ++f)
#pragma unroll
    for (int t = 0; t < 2; ++t)
      st.acc[f][t] = __builtin_amdgcn_mfma_f32_16x16x32_bf16(st.wf[S & 1][f], st.hf[S & 3][t], st.acc[f][t], 0, 0, 0);
  SB();
  if constexpr (S + 1 < NS) g2_step<S + 1>(st, ph, pw, smem, tid, wave, lw);
}

DEVI void g2_phase(char* __restrict__ ws, __amdgpu_buffer_rsrc_t rs_ws, char* smem, int L, int sp, int mt, int nb2) {
  THREAD_IDS();
  const int kg = wave >> 2, wt = wave & 3;
  const char* ph = ws + OFF_H + ((size_t)((mt * 8 + 2 * wt) * 144 + sp * 36 + kg) * 64 + lane) * 16;
  const char* pw = ws + OFF_W2 + (size_t)(nb2 * 144 + sp * 36) * 9216;
  const char* lw = smem + kg * 9216 + lane * 16;
  g2_issue_w<0>(pw, smem, tid, wave);
  g2_issue_w<1>(pw + 18432, smem, tid, wave);
  g2_issue_w<2>(pw + 2 * 18432, smem, tid, wave);
  wait_cnt(ws, C_H + L * 32 + (mt >> 1) * 4 + sp, 8);
  G2State st;
  g2_issue_h<0>(st, ph);
  g2_issue_h<1>(st, ph + 2048);
  g2_issue_h<2>(st, ph + 4096);
#pragma unroll
  for (int f = 0; f < 9; ++f)
#pragma unroll
    for (int t = 0; t < 2; ++t) st.acc[f][t] = f32x4{0.f, 0.f, 0.f, 0.f};
  asm volatile("s_waitcnt vmcnt(4)" : "+v"(st.hf[0][0]), "+v"(st.hf[0][1]) :: "memory");
  bar();
#pragma unroll
  for (int f = 0; f < 9; ++f) st.wf[0][f] = *(const bf16x8*)(lw + f * 1024);
  g2_step<0>(st, ph, pw, smem, tid, wave, lw);
  __syncthreads();
  char* xb = smem + (wt * 18) * 1024 + lane * 16;
  if (kg == 1) {
#pragma unroll
    for (int f = 0; f < 9; ++f)
#pragma unroll
      for (int t = 0; t < 2; ++t) *(f32x4*)(xb + (f * 2 + t) * 1024) = st.acc[f][t];
  }
  __syncthreads();
  if (kg == 0) {
#pragma unroll
    for (int f = 0; f < 9; ++f)
#pragma unroll
      for (int t = 0; t < 2; ++t) {
        f32x4 o = st.acc[f][t] + *(const f32x4*)(xb + (f * 2 + t) * 1024);
        const int T = mt * 8 + wt * 2 + t;
        unsigned off = (unsigned)(OFF_P + ((size_t)((sp * 128 + T) * 36 + nb2 * 9 + f) * 64 + lane) * 16);
        st16(rs_ws, off, __builtin_bit_cast(u32x4, o));
      }
  }
  signal_cnt(ws, C_P + L * 16 + mt);
}

__global__ __launch_bounds__(512, 2) void k_ffn(const u16* __restrict__ q, char* __restrict__ ws, u16* __restrict__ out) {
  extern __shared__ __attribute__((aligned(16))) char smem[];
  const int bid = blockIdx.x, xcd = bid & 7, idx = bid >> 3;
  const int mb = (xcd & 1) * 4 + (idx & 3), nb = (xcd >> 1) * 8 + (idx >> 2);
  const int sp = xcd >> 1, mt = (xcd & 1) * 8 + (idx & 7), nb2 = idx >> 3;
  const int ui = sp * 4 + nb2;
  const int T = mt * 8 + (ui >> 1), half = ui & 1;
  __amdgpu_buffer_rsrc_t rs_ws = __builtin_amdgcn_make_buffer_rsrc(ws, 0, 0x10000000, 0x00020000);

  STAMP(4, 0);
  row_phase<0>(q, ws, rs_ws, out, smem, T, half);
  signal_cnt(ws, C_XN + (T >> 4));
  STAMP(4, 1);
  g1_phase(ws, rs_ws, smem, 0, mb, nb);
  STAMP(4, 2);
  g2_phase(ws, rs_ws, smem, 0, sp, mt, nb2);
  STAMP(4, 3);
  wait_cnt(ws, C_P + mt, 16);
  row_phase<1>(q, ws, rs_ws, out, smem, T, half);
  signal_cnt(ws, C_XN + 8 + (T >> 4));
  STAMP(4, 4);
  g1_phase(ws, rs_ws, smem, 1, mb, nb);
  STAMP(4, 5);
  g2_phase(ws, rs_ws, smem, 1, sp, mt, nb2);
  STAMP(4, 6);
  wait_cnt(ws, C_P + 16 + mt, 16);
  row_phase<2>(q, ws, rs_ws, out, smem, T, half);
  STAMP(4, 7);
}

extern "C" void kernel_launch(void* const* d_in, const int* in_sizes, int n_in, void* d_out, int out_size, void* d_ws,
                              size_t ws_size, hipStream_t stream) {
  const u16* q = (const u16*)d_in[0];
  const u16* kin = (const u16*)d_in[1];
  const u16* vin = (const u16*)d_in[2];
  const u16* shiftk = (const u16*)d_in[3];
  const u16* ssmk = (const u16*)d_in[4];
  const u16* ssmD = (const u16*)d_in[5];
  const u16* w1 = (const u16*)d_in[7];
  const u16* w3 = (const u16*)d_in[8];
  const u16* w2 = (const u16*)d_in[9];
  char* ws = (char*)d_ws;
  u16* out = (u16*)d_out;

  (void)hipFuncSetAttribute((const void*)k_conv, hipFuncAttributeMaxDynamicSharedMemorySize, PREP_LDS);
  (void)hipFuncSetAttribute((const void*)k_ffn, hipFuncAttributeMaxDynamicSharedMemorySize, GEMM_LDS);

  k_pre<<<(NT_KVT + NT_RET) / 256, 256, 0, stream>>>(kin, vin, shiftk, w1, w3, w2, ws);
  k_conv<<<144, 1024, PREP_LDS, stream>>>(ssmk, ssmD, ws);
  k_ffn<<<256, 512, GEMM_LDS, stream>>>(q, ws, out);
}
```

```cpp
#include <hip/hip_runtime.h>
#include <hip/hip_bf16.h>
#include <stdint.h>

typedef __attribute__((ext_vector_type(8))) short bf16x8;
typedef __attribute__((ext_vector_type(4))) float f32x4;
typedef __attribute__((ext_vector_type(16))) float f32x16;
typedef __attribute__((ext_vector_type(4))) unsigned int u32x4;
typedef __attribute__((ext_vector_type(2))) unsigned int u32x2;
typedef __attribute__((ext_vector_type(2))) __bf16 bf16x2_t;
typedef __attribute__((ext_vector_type(2))) float f32x2;
typedef unsigned short u16;

#define AS1 __attribute__((address_space(1)))
#define AS3 __attribute__((address_space(3)))
#define DEVI __device__ __forceinline__

constexpr size_t SZ_W13 = 576ull * 18 * 1024;
constexpr size_t SZ_W2 = 36ull * 144 * 1024;
constexpr size_t SZ_ACT = 2048ull * 576 * 2;
constexpr size_t SZ_H = 128ull * 144 * 1024;
constexpr size_t SZ_P = 4ull * 128 * 36 * 1024;
constexpr size_t OFF_W13 = 0;
constexpr size_t OFF_W2 = OFF_W13 + SZ_W13;
constexpr size_t OFF_Y = OFF_W2 + SZ_W2;
constexpr size_t OFF_X1 = OFF_Y + SZ_ACT;
constexpr size_t OFF_X3 = OFF_X1 + SZ_ACT;
constexpr size_t OFF_XN = OFF_X3 + SZ_ACT;
constexpr size_t OFF_H = OFF_XN + SZ_ACT;
constexpr size_t OFF_P = OFF_H + SZ_H;
constexpr size_t OFF_KVT = OFF_P + SZ_P;

DEVI unsigned pack_bf16(float lo, float hi) {
  f32x2 v = {lo, hi};
  bf16x2_t r = __builtin_convertvector(v, bf16x2_t);
  return __builtin_bit_cast(unsigned, r);
}
DEVI float bflo(unsigned u) { return __uint_as_float(u << 16); }
DEVI float bfhi(unsigned u) { return __uint_as_float(u & 0xffff0000u); }
DEVI float rbf(float f) { return bflo(pack_bf16(f, f)); }

template <int N> DEVI void wait_vm() { asm volatile("s_waitcnt vmcnt(%0)" ::"n"(N) : "memory"); }
DEVI void bar() { __builtin_amdgcn_s_barrier(); }

#define STAMP(kk, id) do {} while (0)

DEVI void glds16(const void* g, void* l) {
  __builtin_amdgcn_global_load_lds((const AS1 void*)g, (AS3 void*)l, 16, 0, 0);
}
DEVI void glds4(const void* g, void* l) {
  __builtin_amdgcn_global_load_lds((const AS1 void*)g, (AS3 void*)l, 4, 0, 0);
}

#define SB() __builtin_amdgcn_sched_barrier(0)
#define WAIT_LGKM0() __builtin_amdgcn_s_waitcnt(0xC07F)
constexpr int HC_STRIDE = 260 * 16;
constexpr int HC_CH = 8 * HC_STRIDE;
constexpr int UB_S = 98;
constexpr int UB_CH = 4 * UB_S * 16;
constexpr int PREP_LDS = 4 * HC_CH + 4 * UB_CH;

DEVI float chsel(u32x2 r, int ch) { return ch == 0 ? bflo(r.x) : ch == 1 ? bfhi(r.x) : ch == 2 ? bflo(r.y) : bfhi(r.y); }

DEVI void transpose8(const u32x4 (&r)[8], u32x4 (&o)[8]) {
#pragma unroll
  for (int c = 0; c < 8; ++c) {
    const unsigned sel = (c & 1) ? 0x07060302u : 0x05040100u;
    unsigned d[4];
#pragma unroll
    for (int i = 0; i < 4; ++i) d[i] = __builtin_amdgcn_perm(r[2 * i + 1][c >> 1], r[2 * i][c >> 1], sel);
    o[c] = u32x4{d[0], d[1], d[2], d[3]};
  }
}

constexpr int NW_RET13 = 72 * 18, NW_RET2 = 9 * 72, NW_RET = NW_RET13 + NW_RET2;
constexpr int RET_FS = 1024 + 16;
DEVI void retile_wave(const u16* __restrict__ w1, const u16* __restrict__ w3, const u16* __restrict__ w2,
                      char* __restrict__ ws, char* lds, int wt, int lane) {
  u32x4 r[8], o[8];
  if (wt < NW_RET13) {
    const int fgg = wt % 72, kt = wt / 72;
    const int mat = lane >> 5, G = (lane >> 3) & 3, fgl = lane & 7;
    const int fg = fgg * 8 + fgl;
    const u16* src = (mat ? w3 : w1) + (size_t)(kt * 32 + G * 8) * 4608 + fg * 8;
#pragma unroll
    for (int i = 0; i < 8; ++i) r[i] = *(const u32x4*)(src + (size_t)i * 4608);
    transpose8(r, o);
    char* d = lds + fgl * RET_FS + (G * 16 + mat * 2) * 16;
#pragma unroll
    for (int c = 0; c < 8; ++c) *(u32x4*)(d + (4 * (c >> 1) + (c & 1)) * 16) = o[c];
    asm volatile("s_waitcnt lgkmcnt(0)" ::: "memory");
#pragma unroll
    for (int n = 0; n < 8; ++n) {
      const int f2 = fgg * 8 + n;
      const int nb = f2 / 18, ftl = f2 % 18;
      u32x4 v = *(const u32x4*)(lds + n * RET_FS + lane * 16);
      *(u32x4*)(ws + OFF_W13 + ((size_t)((nb * 18 + kt) * 18 + ftl) * 64 + lane) * 16) = v;
    }
  } else {
    const int w2t = wt - NW_RET13;
    const int mg = w2t % 9, kp2 = w2t / 9;
    const int G = lane >> 4, kpar = (lane >> 3) & 1, ogl = lane & 7;
    const int kt2 = kp2 * 2 + kpar, og = mg * 8 + ogl;
    const u16* src = w2 + (size_t)(kt2 * 32 + 2 * G) * 576 + og * 8;
#pragma unroll
    for (int i = 0; i < 8; ++i) r[i] = *(const u32x4*)(src + (size_t)(8 * (i >> 1) + (i & 1)) * 576);
    transpose8(r, o);
    const int g = ogl & 3;
#pragma unroll
    for (int e = 0; e < 2; ++e) {
      char* d = lds + (kpar * 4 + (ogl >> 2) * 2 + e) * RET_FS + (G * 16 + 4 * g) * 16;
#pragma unroll
      for (int c = 0; c < 4; ++c) *(u32x4*)(d + c * 16) = o[4 * e + c];
    }
    asm volatile("s_waitcnt lgkmcnt(0)" ::: "memory");
#pragma unroll
    for (int n = 0; n < 8; ++n) {
      const int k2 = kp2 * 2 + (n >> 2), ot = (mg * 2 + ((n >> 1) & 1)) * 2 + (n & 1);
      const int nb2 = ot / 9, otl = ot % 9;
      u32x4 v = *(const u32x4*)(lds + n * RET_FS + lane * 16);
      *(u32x4*)(ws + OFF_W2 + ((size_t)((nb2 * 144 + k2) * 9 + otl) * 64 + lane) * 16) = v;
    }
  }
  asm volatile("s_waitcnt lgkmcnt(0)" ::: "memory");
}

__global__ __launch_bounds__(1024, 4) void k_conv(const u16* __restrict__ ssmk, const u16* __restrict__ ssmD,
                                                 const u16* __restrict__ w1, const u16* __restrict__ w3,
                                                 const u16* __restrict__ w2, char* __restrict__ ws) {
  extern __shared__ __attribute__((aligned(16))) char smem[];
  if (blockIdx.x >= 144) {
    const int wv = __builtin_amdgcn_readfirstlane(threadIdx.x >> 6);
    for (int wt = (blockIdx.x - 144) * 16 + wv; wt < NW_RET; wt += 112 * 16)
      retile_wave(w1, w3, w2, ws, smem + wv * (8 * RET_FS), wt, threadIdx.x & 63);
    return;
  }
  const int tid = threadIdx.x, lane = tid & 63;
  const int wave = __builtin_amdgcn_readfirstlane(tid >> 6);
  const int cg = blockIdx.x;
  const int d0 = cg * 4;
  const int s0 = tid * 2;
  char* ubase = smem + 4 * HC_CH;

  STAMP(3, 0);
  {
    const int ch = wave & 3, b8 = wave >> 2;
    glds16(ws + OFF_KVT + (size_t)(d0 + ch) * 4096 + (4 * lane + b8) * 16, ubase + ch * UB_CH + b8 * (UB_S * 16));
  }
  {
    const int ch = wave & 3, qq = wave >> 2;
    const u16* hsrc = ssmk + (size_t)(d0 + ch) * 2048;
    char* hb = smem + ch * HC_CH;
#pragma unroll
    for (int n2 = 0; n2 < 2; ++n2) {
      int cp = -4 + lane + 64 * (n2 == 0 ? qq : 4);
      bool act = (n2 == 0) || (qq == 0 && cp <= 254);
      if (act) {
        int c0 = cp < 0 ? 0 : cp, c1 = cp + 1 < 0 ? 0 : cp + 1;
        u32x4 lo = *(const u32x4*)(hsrc + 8 * c0);
        u32x4 hi = *(const u32x4*)(hsrc + 8 * c1);
        u32x4 z = {0u, 0u, 0u, 0u};
        if (cp < 0) lo = z;
        if (cp + 1 < 0) hi = z;
        unsigned Dw[8] = {lo.x, lo.y, lo.z, lo.w, hi.x, hi.y, hi.z, hi.w};
        unsigned Ow[7];
#pragma unroll
        for (int t = 0; t < 7; ++t) Ow[t] = (Dw[t] >> 16) | (Dw[t + 1] << 16);
        char* dst = hb + (cp + 4) * 16;
#pragma unroll
        for (int p = 0; p < 8; ++p) {
          int pp = p + 1;
          u32x4 o;
          if (pp & 1) {
            int e = (pp - 1) >> 1;
            o = u32x4{Ow[e], Ow[e + 1], Ow[e + 2], Ow[e + 3]};
          } else {
            int e = pp >> 1;
            o = u32x4{Dw[e], Dw[e + 1], Dw[e + 2], Dw[e + 3]};
          }
          *(u32x4*)(dst + p * HC_STRIDE) = o;
        }
      }
    }
  }
  if (tid < 512) {
    int ch = tid >> 7, rem = tid & 127;
    u32x4 z = {0u, 0u, 0u, 0u};
    *(u32x4*)(ubase + ch * UB_CH + (((rem >> 5) * UB_S) + 64 + (rem & 31)) * 16) = z;
  }
  wait_vm<0>();
  __syncthreads();
  STAMP(3, 1);

  if (wave < 4) {
    f32x16 acc0 = {}, acc1 = {};
    const int i = lane & 31, hh = lane >> 5;
    const char* pa = smem + wave * HC_CH + (lane & 7) * HC_STRIDE + (hh + (i >> 3)) * 16;
    const char* pb1 = ubase + wave * UB_CH + hh * (UB_S * 16) + (31 - i) * 16;
    const char* pb0 = pb1 + 32 * 16;
    bf16x8 a[2][2], b1[2][2], b0[2][2];
#define CONV_LD(buf, q, full)                                                    \
  {                                                                              \
    _Pragma("unroll") for (int e = 0; e < 2; ++e) {                              \
      a[buf][e] = *(const bf16x8*)(pa + (2 * (q) + e) * 32);                     \
      b1[buf][e] = *(const bf16x8*)(pb1 + e * (2 * UB_S * 16) + (q) * 16);       \
      if (full) b0[buf][e] = *(const bf16x8*)(pb0 + e * (2 * UB_S * 16) + (q) * 16); \
    }                                                                            \
  }
#define CONV_MM(buf, full)                                                                        \
  {                                                                                               \
    _Pragma("unroll") for (int e = 0; e < 2; ++e) {                                               \
      acc1 = __builtin_amdgcn_mfma_f32_32x32x16_bf16(a[buf][e], b1[buf][e], acc1, 0, 0, 0);       \
      if (full) acc0 = __builtin_amdgcn_mfma_f32_32x32x16_bf16(a[buf][e], b0[buf][e], acc0, 0, 0, 0); \
    }                                                                                             \
  }
    CONV_LD(0, 0, true);
    for (int q = 0; q < 32; q += 2) {
      WAIT_LGKM0(); SB();
      CONV_LD(1, q + 1, true);
      SB();
      CONV_MM(0, true);
      WAIT_LGKM0(); SB();
      if (q + 2 < 32) { CONV_LD(0, q + 2, true); } else { CONV_LD(0, q + 2, false); }
      SB();
      CONV_MM(1, true);
    }
    for (int q = 32; q < 64; q += 2) {
      WAIT_LGKM0(); SB();
      CONV_LD(1, q + 1, false);
      SB();
      CONV_MM(0, false);
      WAIT_LGKM0(); SB();
      if (q + 2 < 64) { CONV_LD(0, q + 2, false); }
      SB();
      CONV_MM(1, false);
    }
    STAMP(3, 2);
    char* yb = smem + wave * HC_CH;
#pragma unroll
    for (int q4 = 0; q4 < 4; ++q4) {
      u32x2 o0 = {pack_bf16(acc0[4 * q4], acc0[4 * q4 + 1]), pack_bf16(acc0[4 * q4 + 2], acc0[4 * q4 + 3])};
      u32x2 o1 = {pack_bf16(acc1[4 * q4], acc1[4 * q4 + 1]), pack_bf16(acc1[4 * q4 + 2], acc1[4 * q4 + 3])};
      *(u32x2*)(yb + (32 * i + 8 * q4 + 4 * hh) * 2) = o0;
      *(u32x2*)(yb + (32 * (32 + i) + 8 * q4 + 4 * hh) * 2) = o1;
    }
  }
  __syncthreads();
  {
    u32x2 dd = *(const u32x2*)(ssmD + d0);
    float yv[4][2];
    int cidx = 255 - (tid >> 2);
    int off = ((cidx & 3) * UB_S + (cidx >> 2)) * 16 + (3 - (tid & 3)) * 4;
#pragma unroll
    for (int ch = 0; ch < 4; ++ch) {
      unsigned c = *(const unsigned*)(smem + ch * HC_CH + tid * 4);
      unsigned kvp = *(const unsigned*)(ubase + ch * UB_CH + off);
      float dv = chsel(dd, ch);
      yv[ch][0] = rbf(bflo(c) + rbf(bfhi(kvp) * dv));
      yv[ch][1] = rbf(bfhi(c) + rbf(bflo(kvp) * dv));
    }
    char* yo = ws + OFF_Y + ((size_t)cg * 2048 + s0) * 8;
    u32x4 o = {pack_bf16(yv[0][0], yv[1][0]), pack_bf16(yv[2][0], yv[3][0]), pack_bf16(yv[0][1], yv[1][1]),
               pack_bf16(yv[2][1], yv[3][1])};
    *(u32x4*)yo = o;
  }
  STAMP(3, 3);
}

DEVI void kvt_part(const u16* __restrict__ kin, const u16* __restrict__ vin, const u16* __restrict__ shiftk,
                   char* __restrict__ ws, int id) {
  const int lane = id & 63, wv = id >> 6;
  const int cgp = wv % 9, tt = wv / 9;
  const int tb = lane >> 3, cc = lane & 7;
  const int s0 = tt * 32 + tb * 4, c0 = cgp * 64 + cc * 8;
  u32x4 kr[7], vr[4], wr[4];
#pragma unroll
  for (int i = 0; i < 7; ++i) {
    int s = s0 - 3 + i;
    s = s < 0 ? 0 : s;
    kr[i] = *(const u32x4*)(kin + (size_t)s * 576 + c0);
  }
#pragma unroll
  for (int i = 0; i < 4; ++i) vr[i] = *(const u32x4*)(vin + (size_t)(s0 + i) * 576 + c0);
#pragma unroll
  for (int i = 0; i < 4; ++i) wr[i] = *(const u32x4*)(shiftk + (size_t)c0 * 4 + i * 8);
  if (s0 == 0) {
    u32x4 z = {0u, 0u, 0u, 0u};
    kr[0] = z; kr[1] = z; kr[2] = z;
  }
#pragma unroll
  for (int ch = 0; ch < 8; ++ch) {
    unsigned w01 = wr[ch >> 1][(ch & 1) * 2], w23 = wr[ch >> 1][(ch & 1) * 2 + 1];
    float w[4] = {bflo(w01), bfhi(w01), bflo(w23), bfhi(w23)};
    float kv[4];
#pragma unroll
    for (int i = 0; i < 4; ++i) {
      float acc = 0.f;
#pragma unroll
      for (int j = 3; j >= 0; --j) {
        unsigned kw = kr[i + 3 - j][ch >> 1];
        acc = rbf(acc + rbf(((ch & 1) ? bfhi(kw) : bflo(kw)) * w[j]));
      }
      unsigned vw = vr[i][ch >> 1];
      kv[i] = acc * ((ch & 1) ? bfhi(vw) : bflo(vw));
    }
    u32x2 o = {pack_bf16(kv[3], kv[2]), pack_bf16(kv[1], kv[0])};
    *(u32x2*)(ws + OFF_KVT + ((size_t)(c0 + ch) * 2048 + 2044 - s0) * 2) = o;
  }
}

constexpr int NT_KVT = 2048 * 576 / 32;
__global__ __launch_bounds__(256) void k_pre(const u16* __restrict__ kin, const u16* __restrict__ vin,
                                             const u16* __restrict__ shiftk, char* __restrict__ ws) {
  const int id = blockIdx.x * 256 + threadIdx.x;
  kvt_part(kin, vin, shiftk, ws, id);
}

DEVI void layernorm8(const float (&x)[8], float (&o)[8], float2* red, int wave, int t8) {
  float m = 0.f;
#pragma unroll
  for (int j = 0; j < 8; ++j) m += x[j];
  m *= 0.125f;
  float M2 = 0.f;
#pragma unroll
  for (int j = 0; j < 8; ++j) {
    float d = x[j] - m;
    M2 += d * d;
  }
  float n2 = 4.f;
#pragma unroll
  for (int sh = 8; sh <= 32; sh <<= 1) {
    float mo = __shfl_xor(m, sh), M2o = __shfl_xor(M2, sh);
    float d = mo - m;
    M2 = M2 + M2o + d * d * n2;
    m = 0.5f * (m + mo);
    n2 *= 2.f;
  }
  if ((threadIdx.x & 63) < 8) red[wave * 8 + t8] = make_float2(m, M2);
  __syncthreads();
  float2 pr[9];
  float mean = 0.f;
#pragma unroll
  for (int w = 0; w < 9; ++w) {
    pr[w] = red[w * 8 + t8];
    mean += pr[w].x;
  }
  mean *= (1.0f / 9.0f);
  float tq = 0.f;
#pragma unroll
  for (int w = 0; w < 9; ++w) {
    float d = pr[w].x - mean;
    tq += pr[w].y + 64.f * d * d;
  }
  float var = rbf(tq * (1.0f / 576.0f));
  float mb = rbf(mean);
  float sd = rbf(__builtin_amdgcn_sqrtf(rbf(var + 1e-5f)));
  float rs = __builtin_amdgcn_rcpf(sd);
#pragma unroll
  for (int j = 0; j < 8; ++j) o[j] = rbf(rbf(x[j] - mb) * rs);
}

template <int MODE>
__global__ __launch_bounds__(576) void k_row(const u16* __restrict__ q, char* __restrict__ ws, u16* __restrict__ out) {
  __shared__ float2 red[2][72];
  const int tid = threadIdx.x, lane = tid & 63, wave = tid >> 6;
  const int T = blockIdx.x >> 1, half = blockIdx.x & 1;
  const int kp = lane >> 5, G = (lane >> 3) & 3, t8 = lane & 7;
  const int tok = half * 8 + t8, token = T * 16 + tok;
  const int kt = 2 * wave + kp;
  const int chunk = G * 16 + tok;
  const size_t fragoff = ((size_t)(T * 18 + kt) * 64 + chunk) * 16;

  float x[8];
  if (MODE == 0) {
    u32x4 c = *(const u32x4*)(q + (size_t)token * 576 + kt * 32 + G * 8);
    x[0] = bflo(c.x); x[1] = bfhi(c.x); x[2] = bflo(c.y); x[3] = bfhi(c.y);
    x[4] = bflo(c.z); x[5] = bfhi(c.z); x[6] = bflo(c.w); x[7] = bfhi(c.w);
  } else {
    f32x4 pa = {0.f, 0.f, 0.f, 0.f}, pb = {0.f, 0.f, 0.f, 0.f};
#pragma unroll
    for (int sp = 0; sp < 4; ++sp) {
      const char* pp = ws + OFF_P + ((size_t)((sp * 128 + T) * 36 + 2 * kt) * 64 + chunk) * 16;
      pa += *(const f32x4*)pp;
      pb += *(const f32x4*)(pp + 1024);
    }
    u32x4 c = *(const u32x4*)(ws + (MODE == 1 ? OFF_X1 : OFF_X3) + fragoff);
    float xp[8] = {bflo(c.x), bfhi(c.x), bflo(c.y), bfhi(c.y), bflo(c.z), bfhi(c.z), bflo(c.w), bfhi(c.w)};
#pragma unroll
    for (int j = 0; j < 4; ++j) {
      x[j] = rbf(xp[j] + rbf(pa[j]));
      x[4 + j] = rbf(xp[4 + j] + rbf(pb[j]));
    }
  }
  if (MODE == 2) {
    u32x4 o = {pack_bf16(x[0], x[1]), pack_bf16(x[2], x[3]), pack_bf16(x[4], x[5]), pack_bf16(x[6], x[7])};
    *(u32x4*)(out + (size_t)token * 576 + kt * 32 + G * 8) = o;
    return;
  }
  u32x2 ya = *(const u32x2*)(ws + OFF_Y + ((size_t)(8 * kt + 2 * G) * 2048 + token) * 8);
  u32x2 yb = *(const u32x2*)(ws + OFF_Y + ((size_t)(8 * kt + 2 * G + 1) * 2048 + token) * 8);
  float yv[8] = {bflo(ya.x), bfhi(ya.x), bflo(ya.y), bfhi(ya.y), bflo(yb.x), bfhi(yb.x), bflo(yb.y), bfhi(yb.y)};
  float a[8];
  layernorm8(x, a, red[0], wave, t8);
  float xn[8];
#pragma unroll
  for (int j = 0; j < 8; ++j) xn[j] = rbf(x[j] + rbf(a[j] * yv[j]));
  {
    u32x4 o = {pack_bf16(xn[0], xn[1]), pack_bf16(xn[2], xn[3]), pack_bf16(xn[4], xn[5]), pack_bf16(xn[6], xn[7])};
    *(u32x4*)(ws + (MODE == 0 ? OFF_X1 : OFF_X3) + fragoff) = o;
  }
  float b[8];
  layernorm8(xn, b, red[1], wave, t8);
  {
    u32x4 o = {pack_bf16(b[0], b[1]), pack_bf16(b[2], b[3]), pack_bf16(b[4], b[5]), pack_bf16(b[6], b[7])};
    size_t off = ((size_t)(((T >> 4) * 18 + kt) * 16 + (T & 15)) * 64 + chunk) * 16;
    *(u32x4*)(ws + OFF_XN + off) = o;
  }
}

constexpr int STAGE_B = 34 * 1024;
constexpr int NSLOT = 4;
constexpr int GEMM_LDS = NSLOT * STAGE_B;

struct G1State {
  f32x4 acc[9][4];
  bf16x8 xf[2][4];
  bf16x8 wf[9];
};

DEVI void g1_issue(const char* px, const char* pw, char* smem, int slot, int tid, int wave) {
  char* l = smem + slot * STAGE_B;
  glds16(px + tid * 16, l + wave * 1024);
  glds16(px + 8192 + tid * 16, l + 8192 + wave * 1024);
  glds16(pw + tid * 16, l + 16384 + wave * 1024);
  glds16(pw + 8192 + tid * 16, l + 24576 + wave * 1024);
  glds4(pw + 16384 + tid * 4, l + 32768 + wave * 256);
}

template <int S>
DEVI void g1_step(G1State& st, const char* px, const char* pw, char* smem, int tid, int wave, const char* lx,
                  const char* lw) {
  constexpr int NS = 18;
  if (S + 1 < NS) {
    wait_vm<(S + 2 < NS) ? 5 : 0>();
    bar();
  }
  WAIT_LGKM0();
  SB();
  const char* cw = lw + (S % NSLOT) * STAGE_B;
#pragma unroll
  for (int f = 5; f < 9; ++f) st.wf[f] = *(const bf16x8*)(cw + f * 1024);
  if (S + 3 < NS) g1_issue(px + (S + 3) * 16384, pw + (S + 3) * 18432, smem, (S + 3) % NSLOT, tid, wave);
  SB();
#pragma unroll
  for (int f = 0; f < 5; ++f)
#pragma unroll
    for (int t = 0; t < 4; ++t)
      st.acc[f][t] = __builtin_amdgcn_mfma_f32_16x16x32_bf16(st.wf[f], st.xf[S & 1][t], st.acc[f][t], 0, 0, 0);
  WAIT_LGKM0();
  SB();
  if (S + 1 < NS) {
    const char* nx = lx + ((S + 1) % NSLOT) * STAGE_B;
#pragma unroll
    for (int t = 0; t < 4; ++t) st.xf[(S + 1) & 1][t] = *(const bf16x8*)(nx + t * 1024);
    const char* nw = lw + ((S + 1) % NSLOT) * STAGE_B;
#pragma unroll
    for (int f = 0; f < 5; ++f) st.wf[f] = *(const bf16x8*)(nw + f * 1024);
  }
  SB();
#pragma unroll
  for (int f = 5; f < 9; ++f)
#pragma unroll
    for (int t = 0; t < 4; ++t)
      st.acc[f][t] = __builtin_amdgcn_mfma_f32_16x16x32_bf16(st.wf[f], st.xf[S & 1][t], st.acc[f][t], 0, 0, 0);
  SB();
  if constexpr (S + 1 < NS) g1_step<S + 1>(st, px, pw, smem, tid, wave, lx, lw);
}

DEVI float silu_f(float g) { return g * __builtin_amdgcn_rcpf(1.0f + __builtin_amdgcn_exp2f(g * -1.44269504088896f)); }

typedef __attribute__((ext_vector_type(3))) unsigned int u32x3;
template <int BM4>
DEVI void g1_epilogue(const G1State& st, char* hb) {
#pragma unroll
  for (int t = 0; t < 4; ++t) {
    unsigned d[9];
#pragma unroll
    for (int f = 0; f < 9; ++f) {
      f32x2 g = {st.acc[f][t][0], st.acc[f][t][1]};
      f32x2 u = {st.acc[f][t][2], st.acc[f][t][3]};
      f32x2 e = g * -1.44269504088896f;
      e[0] = __builtin_amdgcn_exp2f(e[0]);
      e[1] = __builtin_amdgcn_exp2f(e[1]);
      e = e + 1.0f;
      e[0] = __builtin_amdgcn_rcpf(e[0]);
      e[1] = __builtin_amdgcn_rcpf(e[1]);
      f32x2 h = (g * e) * u;
      d[f] = pack_bf16(h[0], h[1]);
    }
    char* p = hb + (size_t)t * 144 * 1024;
    if (BM4 == 0) {
      *(u32x4*)(p) = u32x4{d[0], d[1], d[2], d[3]};
      *(u32x4*)(p + 1024) = u32x4{d[4], d[5], d[6], d[7]};
      *(unsigned*)(p + 2048) = d[8];
    } else if (BM4 == 1) {
      *(u32x3*)(p + 4) = u32x3{d[0], d[1], d[2]};
      *(u32x4*)(p + 1024) = u32x4{d[3], d[4], d[5], d[6]};
      *(u32x2*)(p + 2048) = u32x2{d[7], d[8]};
    } else if (BM4 == 2) {
      *(u32x2*)(p + 8) = u32x2{d[0], d[1]};
      *(u32x4*)(p + 1024) = u32x4{d[2], d[3], d[4], d[5]};
      *(u32x3*)(p + 2048) = u32x3{d[6], d[7], d[8]};
    } else {
      *(unsigned*)(p + 12) = d[0];
      *(u32x4*)(p + 1024) = u32x4{d[1], d[2], d[3], d[4]};
      *(u32x4*)(p + 2048) = u32x4{d[5], d[6], d[7], d[8]};
    }
  }
}

__global__ __launch_bounds__(512, 2) void k_gemm1(char* __restrict__ ws) {
  extern __shared__ __attribute__((aligned(16))) char smem[];
  const int tid = threadIdx.x, lane = tid & 63;
  const int wave = __builtin_amdgcn_readfirstlane(tid >> 6);
  const int bid = blockIdx.x, xcd = bid & 7, idx = bid >> 3;
  const int mb = (xcd & 1) * 4 + (idx & 3), nb = (xcd >> 1) * 8 + (idx >> 2);
  const int wm = wave & 3, wn = wave >> 2;
  const char* px = ws + OFF_XN + (size_t)mb * 18 * 16384;
  const char* pw = ws + OFF_W13 + (size_t)nb * 18 * 18432;
  const char* lx = smem + wm * 4096 + lane * 16;
  const char* lw = smem + 16384 + wn * 9216 + lane * 16;

  STAMP(1, 0);
  G1State st;
#pragma unroll
  for (int f = 0; f < 9; ++f)
#pragma unroll
    for (int t = 0; t < 4; ++t) st.acc[f][t] = f32x4{0.f, 0.f, 0.f, 0.f};

  g1_issue(px, pw, smem, 0, tid, wave);
  g1_issue(px + 16384, pw + 18432, smem, 1, tid, wave);
  g1_issue(px + 2 * 16384, pw + 2 * 18432, smem, 2, tid, wave);
  wait_vm<10>();
  bar();
  STAMP(1, 1);
#pragma unroll
  for (int t = 0; t < 4; ++t) st.xf[0][t] = *(const bf16x8*)(lx + t * 1024);
#pragma unroll
  for (int f = 0; f < 5; ++f) st.wf[f] = *(const bf16x8*)(lw + f * 1024);
  g1_step<0>(st, px, pw, smem, tid, wave, lx, lw);

  STAMP(1, 2);
  char* hout = ws + OFF_H;
  {
    const int base = nb * 18 + wn * 9;
    char* hb = hout + ((size_t)((mb * 16 + wm * 4) * 144 + (base >> 2)) * 64 + lane) * 16;
    switch (base & 3) {
      case 0: g1_epilogue<0>(st, hb); break;
      case 1: g1_epilogue<1>(st, hb); break;
      case 2: g1_epilogue<2>(st, hb); break;
      default: g1_epilogue<3>(st, hb); break;
    }
  }
  STAMP(1, 3);
}

constexpr int STAGE2_B = 18 * 1024;
struct G2State {
  f32x4 acc[9][2];
  bf16x8 hf[4][2];
  bf16x8 wf[2][9];
};

DEVI void gload16(bf16x8& d, const char* p) {
  asm volatile("global_load_dwordx4 %0, %1, off" : "=v"(d) : "v"(p) : "memory");
}

template <int R>
DEVI void g2_issue(G2State& st, const char* ph, const char* pw, char* smem, int tid, int wave) {
  char* l = smem + R * STAGE2_B;
  glds16(pw + tid * 16, l + wave * 1024);
  glds16(pw + 8192 + tid * 16, l + 8192 + wave * 1024);
  glds4(pw + 16384 + tid * 4, l + 16384 + wave * 256);
  gload16(st.hf[R][0], ph);
  gload16(st.hf[R][1], ph + 144 * 1024);
}

template <int S>
DEVI void g2_step(G2State& st, const char* ph, const char* pw, char* smem, int tid, int wave, const char* lw) {
  constexpr int NS = 18;
  if (S + 1 < NS) {
    asm volatile("s_waitcnt vmcnt(%2)" : "+v"(st.hf[(S + 1) & 3][0]), "+v"(st.hf[(S + 1) & 3][1]) : "n"((S + 2 < NS) ? 5 : 0) : "memory");
    bar();
  }
  WAIT_LGKM0();
  SB();
  if (S + 1 < NS) {
    const char* cw = lw + ((S + 1) % NSLOT) * STAGE2_B;
#pragma unroll
    for (int f = 0; f < 9; ++f) st.wf[(S + 1) & 1][f] = *(const bf16x8*)(cw + f * 1024);
  }
  if (S + 3 < NS) g2_issue<(S + 3) & 3>(st, ph + (S + 3) * 2048, pw + (S + 3) * 18432, smem, tid, wave);
  SB();
#pragma unroll
  for (int f = 0; f < 9; ++f)
#pragma unroll
    for (int t = 0; t < 2; ++t)
      st.acc[f][t] = __builtin_amdgcn_mfma_f32_16x16x32_bf16(st.wf[S & 1][f], st.hf[S & 3][t], st.acc[f][t], 0, 0, 0);
  SB();
  if constexpr (S + 1 < NS) g2_step<S + 1>(st, ph, pw, smem, tid, wave, lw);
}

__global__ __launch_bounds__(512, 2) void k_gemm2(char* __restrict__ ws) {
  extern __shared__ __attribute__((aligned(16))) char smem[];
  const int tid = threadIdx.x, lane = tid & 63;
  const int wave = __builtin_amdgcn_readfirstlane(tid >> 6);
  const int bid = blockIdx.x, xcd = bid & 7, idx = bid >> 3;
  const int sp = xcd >> 1, mt = (xcd & 1) * 8 + (idx & 7), nb2 = idx >> 3;
  const int kg = wave >> 2, wt = wave & 3;
  const char* ph = ws + OFF_H + ((size_t)((mt * 8 + 2 * wt) * 144 + sp * 36 + kg) * 64 + lane) * 16;
  const char* pw = ws + OFF_W2 + (size_t)(nb2 * 144 + sp * 36) * 9216;
  const char* lw = smem + kg * 9216 + lane * 16;

  STAMP(2, 0);
  G2State st;
#pragma unroll
  for (int f = 0; f < 9; ++f)
#pragma unroll
    for (int t = 0; t < 2; ++t) st.acc[f][t] = f32x4{0.f, 0.f, 0.f, 0.f};

  g2_issue<0>(st, ph, pw, smem, tid, wave);
  g2_issue<1>(st, ph + 2048, pw + 18432, smem, tid, wave);
  g2_issue<2>(st, ph + 4096, pw + 2 * 18432, smem, tid, wave);
  asm volatile("s_waitcnt vmcnt(10)" : "+v"(st.hf[0][0]), "+v"(st.hf[0][1]) :: "memory");
  bar();
  STAMP(2, 1);
#pragma unroll
  for (int f = 0; f < 9; ++f) st.wf[0][f] = *(const bf16x8*)(lw + f * 1024);
  g2_step<0>(st, ph, pw, smem, tid, wave, lw);
  STAMP(2, 2);
  __syncthreads();
  char* xb = smem + (wt * 18) * 1024 + lane * 16;
  if (kg == 1) {
#pragma unroll
    for (int f = 0; f < 9; ++f)
#pragma unroll
      for (int t = 0; t < 2; ++t) *(f32x4*)(xb + (f * 2 + t) * 1024) = st.acc[f][t];
  }
  __syncthreads();
  if (kg == 0) {
    char* pout = ws + OFF_P;
#pragma unroll
    for (int f = 0; f < 9; ++f)
#pragma unroll
      for (int t = 0; t < 2; ++t) {
        f32x4 o = st.acc[f][t] + *(const f32x4*)(xb + (f * 2 + t) * 1024);
        const int T = mt * 8 + wt * 2 + t;
        *(f32x4*)(pout + ((size_t)((sp * 128 + T) * 36 + nb2 * 9 + f) * 64 + lane) * 16) = o;
      }
  }
  STAMP(2, 3);
}

extern "C" void kernel_launch(void* const* d_in, const int* in_sizes, int n_in, void* d_out, int out_size, void* d_ws,
                              size_t ws_size, hipStream_t stream) {
  const u16* q = (const u16*)d_in[0];
  const u16* kin = (const u16*)d_in[1];
  const u16* vin = (const u16*)d_in[2];
  const u16* shiftk = (const u16*)d_in[3];
  const u16* ssmk = (const u16*)d_in[4];
  const u16* ssmD = (const u16*)d_in[5];
  const u16* w1 = (const u16*)d_in[7];
  const u16* w3 = (const u16*)d_in[8];
  const u16* w2 = (const u16*)d_in[9];
  char* ws = (char*)d_ws;
  u16* out = (u16*)d_out;

  hipFuncSetAttribute((const void*)k_conv, hipFuncAttributeMaxDynamicSharedMemorySize, PREP_LDS);
  hipFuncSetAttribute((const void*)k_gemm1, hipFuncAttributeMaxDynamicSharedMemorySize, GEMM_LDS);
  hipFuncSetAttribute((const void*)k_gemm2, hipFuncAttributeMaxDynamicSharedMemorySize, 4 * STAGE2_B);

  k_pre<<<NT_KVT / 256, 256, 0, stream>>>(kin, vin, shiftk, ws);
  k_conv<<<256, 1024, PREP_LDS, stream>>>(ssmk, ssmD, w1, w3, w2, ws);
  k_row<0><<<256, 576, 0, stream>>>(q, ws, out);
  k_gemm1<<<256, 512, GEMM_LDS, stream>>>(ws);
  k_gemm2<<<256, 512, 4 * STAGE2_B, stream>>>(ws);
  k_row<1><<<256, 576, 0, stream>>>(q, ws, out);
  k_gemm1<<<256, 512, GEMM_LDS, stream>>>(ws);
  k_gemm2<<<256, 512, 4 * STAGE2_B, stream>>>(ws);
  k_row<2><<<256, 576, 0, stream>>>(q, ws, out);
}
```

```cpp
#include <hip/hip_runtime.h>
#include <hip/hip_bf16.h>
#include <stdint.h>

typedef __attribute__((ext_vector_type(8))) short bf16x8;
typedef __attribute__((ext_vector_type(4))) float f32x4;
typedef __attribute__((ext_vector_type(16))) float f32x16;
typedef __attribute__((ext_vector_type(4))) unsigned int u32x4;
typedef __attribute__((ext_vector_type(2))) unsigned int u32x2;
typedef __attribute__((ext_vector_type(2))) __bf16 bf16x2_t;
typedef __attribute__((ext_vector_type(2))) float f32x2;
typedef unsigned short u16;

#define AS1 __attribute__((address_space(1)))
#define AS3 __attribute__((address_space(3)))
#define DEVI __device__ __forceinline__

constexpr size_t SZ_W13 = 576ull * 18 * 1024;
constexpr size_t SZ_W2 = 36ull * 144 * 1024;
constexpr size_t SZ_ACT = 2048ull * 576 * 2;
constexpr size_t SZ_H = 128ull * 144 * 1024;
constexpr size_t SZ_P = 4ull * 128 * 36 * 1024;
constexpr size_t OFF_W13 = 0;
constexpr size_t OFF_W2 = OFF_W13 + SZ_W13;
constexpr size_t OFF_Y = OFF_W2 + SZ_W2;
constexpr size_t OFF_X1 = OFF_Y + SZ_ACT;
constexpr size_t OFF_X3 = OFF_X1 + SZ_ACT;
constexpr size_t OFF_XN = OFF_X3 + SZ_ACT;
constexpr size_t OFF_H = OFF_XN + SZ_ACT;
constexpr size_t OFF_P = OFF_H + SZ_H;
constexpr size_t OFF_KVT = OFF_P + SZ_P;

DEVI unsigned pack_bf16(float lo, float hi) {
  f32x2 v = {lo, hi};
  bf16x2_t r = __builtin_convertvector(v, bf16x2_t);
  return __builtin_bit_cast(unsigned, r);
}
DEVI float bflo(unsigned u) { return __uint_as_float(u << 16); }
DEVI float bfhi(unsigned u) { return __uint_as_float(u & 0xffff0000u); }
DEVI float rbf(float f) { return bflo(pack_bf16(f, f)); }

template <int N> DEVI void wait_vm() { asm volatile("s_waitcnt vmcnt(%0)" ::"n"(N) : "memory"); }
DEVI void bar() { __builtin_amdgcn_s_barrier(); }

#define STAMP(kk, id) do {} while (0)

DEVI void glds16(const void* g, void* l) {
  __builtin_amdgcn_global_load_lds((const AS1 void*)g, (AS3 void*)l, 16, 0, 0);
}
DEVI void glds4(const void* g, void* l) {
  __builtin_amdgcn_global_load_lds((const AS1 void*)g, (AS3 void*)l, 4, 0, 0);
}

DEVI void aglds16(const void* g, unsigned lds) {
  unsigned keep;
  asm volatile("s_mov_b32 %0, m0\n\ts_mov_b32 m0, %2\n\ts_nop 0\n\tglobal_load_lds_dwordx4 %1, off\n\ts_mov_b32 m0, %0"
               : "=&s"(keep) : "v"(g), "s"(lds) : "memory");
}
DEVI void aglds4(const void* g, unsigned lds) {
  unsigned keep;
  asm volatile("s_mov_b32 %0, m0\n\ts_mov_b32 m0, %2\n\ts_nop 0\n\tglobal_load_lds_dword %1, off\n\ts_mov_b32 m0, %0"
               : "=&s"(keep) : "v"(g), "s"(lds) : "memory");
}
DEVI unsigned lds_addr(const void* p) { return (unsigned)(size_t)(const AS3 char*)p; }

#define SB() __builtin_amdgcn_sched_barrier(0)
#define WAIT_LGKM0() __builtin_amdgcn_s_waitcnt(0xC07F)
constexpr int HC_STRIDE = 260 * 16;
constexpr int HC_CH = 8 * HC_STRIDE;
constexpr int UB_S = 98;
constexpr int UB_CH = 4 * UB_S * 16;
constexpr int PREP_LDS = 4 * HC_CH + 4 * UB_CH;

DEVI float chsel(u32x2 r, int ch) { return ch == 0 ? bflo(r.x) : ch == 1 ? bfhi(r.x) : ch == 2 ? bflo(r.y) : bfhi(r.y); }

DEVI void transpose8(const u32x4 (&r)[8], u32x4 (&o)[8]) {
#pragma unroll
  for (int c = 0; c < 8; ++c) {
    const unsigned sel = (c & 1) ? 0x07060302u : 0x05040100u;
    unsigned d[4];
#pragma unroll
    for (int i = 0; i < 4; ++i) d[i] = __builtin_amdgcn_perm(r[2 * i + 1][c >> 1], r[2 * i][c >> 1], sel);
    o[c] = u32x4{d[0], d[1], d[2], d[3]};
  }
}

constexpr int RET_FS = 1024 + 16;
constexpr int NU13 = 18 * 9, NU2 = 144;
constexpr int RET_LDS = 64 * RET_FS;
DEVI void retile_unit(const u16* __restrict__ w1, const u16* __restrict__ w3, const u16* __restrict__ w2,
                      char* __restrict__ ws, char* img, int unit, int tid) {
  u32x4 r[8], o[8];
  const int wave = tid >> 6, lane = tid & 63;
  if (unit >= NU13 + NU2) {
    __syncthreads();
  } else if (unit < NU13) {
    const int kt = unit / 9, fgg = unit % 9;
    const int mat = wave & 1, G = (wave >> 1) & 3;
    const int fg = fgg * 64 + lane;
    const u16* src = (mat ? w3 : w1) + (size_t)(kt * 32 + G * 8) * 4608 + fg * 8;
#pragma unroll
    for (int i = 0; i < 8; ++i) r[i] = *(const u32x4*)(src + (size_t)i * 4608);
    transpose8(r, o);
    char* d = img + lane * RET_FS + (G * 16 + mat * 2) * 16;
#pragma unroll
    for (int c = 0; c < 8; ++c) *(u32x4*)(d + (4 * (c >> 1) + (c & 1)) * 16) = o[c];
    __syncthreads();
#pragma unroll
    for (int n = 0; n < 8; ++n) {
      const int fl = wave * 8 + n;
      const int f2 = fgg * 64 + fl;
      const int nb = f2 / 18, ftl = f2 % 18;
      u32x4 v = *(const u32x4*)(img + fl * RET_FS + lane * 16);
      *(u32x4*)(ws + OFF_W13 + ((size_t)((nb * 18 + kt) * 18 + ftl) * 64 + lane) * 16) = v;
    }
  } else {
    const int kt2 = unit - NU13;
    if (tid < 288) {
      const int G = tid / 72, og = tid % 72;
      const u16* src = w2 + (size_t)(kt2 * 32 + 2 * G) * 576 + og * 8;
#pragma unroll
      for (int i = 0; i < 8; ++i) r[i] = *(const u32x4*)(src + (size_t)(8 * (i >> 1) + (i & 1)) * 576);
      transpose8(r, o);
      const int m = og >> 2, g = og & 3;
#pragma unroll
      for (int e = 0; e < 2; ++e) {
        char* d = img + (2 * m + e) * RET_FS + (G * 16 + 4 * g) * 16;
#pragma unroll
        for (int c = 0; c < 4; ++c) *(u32x4*)(d + c * 16) = o[4 * e + c];
      }
    }
    __syncthreads();
    for (int ot = wave; ot < 36; ot += 8) {
      const int nb2 = ot / 9, otl = ot % 9;
      u32x4 v = *(const u32x4*)(img + ot * RET_FS + lane * 16);
      *(u32x4*)(ws + OFF_W2 + ((size_t)((nb2 * 144 + kt2) * 9 + otl) * 64 + lane) * 16) = v;
    }
  }
  __syncthreads();
}

__global__ __launch_bounds__(1024, 4) void k_conv(const u16* __restrict__ ssmk, const u16* __restrict__ ssmD,
                                                 const u16* __restrict__ w1, const u16* __restrict__ w3,
                                                 const u16* __restrict__ w2, char* __restrict__ ws) {
  extern __shared__ __attribute__((aligned(16))) char smem[];
  if (blockIdx.x >= 144) {
    const int hf = threadIdx.x >> 9;
#pragma unroll 1
    for (int it = 0; it < 2; ++it) {
      const int base = (blockIdx.x - 144) * 2 + it * 224;
      if (base >= NU13 + NU2) break;
      retile_unit(w1, w3, w2, ws, smem + hf * RET_LDS, base + hf, threadIdx.x & 511);
    }
    return;
  }
  const int tid = threadIdx.x, lane = tid & 63;
  const int wave = __builtin_amdgcn_readfirstlane(tid >> 6);
  const int cg = blockIdx.x;
  const int d0 = cg * 4;
  const int s0 = tid * 2;
  char* ubase = smem + 4 * HC_CH;

  STAMP(3, 0);
  {
    const int ch = wave & 3, b8 = wave >> 2;
    glds16(ws + OFF_KVT + (size_t)(d0 + ch) * 4096 + (4 * lane + b8) * 16, ubase + ch * UB_CH + b8 * (UB_S * 16));
  }
  {
    const int ch = wave & 3, qq = wave >> 2;
    const u16* hsrc = ssmk + (size_t)(d0 + ch) * 2048;
    char* hb = smem + ch * HC_CH;
#pragma unroll
    for (int n2 = 0; n2 < 2; ++n2) {
      int cp = -4 + lane + 64 * (n2 == 0 ? qq : 4);
      bool act = (n2 == 0) || (qq == 0 && cp <= 254);
      if (act) {
        int c0 = cp < 0 ? 0 : cp, c1 = cp + 1 < 0 ? 0 : cp + 1;
        u32x4 lo = *(const u32x4*)(hsrc + 8 * c0);
        u32x4 hi = *(const u32x4*)(hsrc + 8 * c1);
        u32x4 z = {0u, 0u, 0u, 0u};
        if (cp < 0) lo = z;
        if (cp + 1 < 0) hi = z;
        unsigned Dw[8] = {lo.x, lo.y, lo.z, lo.w, hi.x, hi.y, hi.z, hi.w};
        unsigned Ow[7];
#pragma unroll
        for (int t = 0; t < 7; ++t) Ow[t] = (Dw[t] >> 16) | (Dw[t + 1] << 16);
        char* dst = hb + (cp + 4) * 16;
#pragma unroll
        for (int p = 0; p < 8; ++p) {
          int pp = p + 1;
          u32x4 o;
          if (pp & 1) {
            int e = (pp - 1) >> 1;
            o = u32x4{Ow[e], Ow[e + 1], Ow[e + 2], Ow[e + 3]};
          } else {
            int e = pp >> 1;
            o = u32x4{Dw[e], Dw[e + 1], Dw[e + 2], Dw[e + 3]};
          }
          *(u32x4*)(dst + p * HC_STRIDE) = o;
        }
      }
    }
  }
  if (tid < 512) {
    int ch = tid >> 7, rem = tid & 127;
    u32x4 z = {0u, 0u, 0u, 0u};
    *(u32x4*)(ubase + ch * UB_CH + (((rem >> 5) * UB_S) + 64 + (rem & 31)) * 16) = z;
  }
  wait_vm<0>();
  __syncthreads();
  STAMP(3, 1);

  if (wave < 4) {
    f32x16 acc0 = {}, acc1 = {};
    const int i = lane & 31, hh = lane >> 5;
    const char* pa = smem + wave * HC_CH + (lane & 7) * HC_STRIDE + (hh + (i >> 3)) * 16;
    const char* pb1 = ubase + wave * UB_CH + hh * (UB_S * 16) + (31 - i) * 16;
    const char* pb0 = pb1 + 32 * 16;
    bf16x8 a[2][2], b1[2][2], b0[2][2];
#define CONV_LD(buf, q, full)                                                    \
  {                                                                              \
    _Pragma("unroll") for (int e = 0; e < 2; ++e) {                              \
      a[buf][e] = *(const bf16x8*)(pa + (2 * (q) + e) * 32);                     \
      b1[buf][e] = *(const bf16x8*)(pb1 + e * (2 * UB_S * 16) + (q) * 16);       \
      if (full) b0[buf][e] = *(const bf16x8*)(pb0 + e * (2 * UB_S * 16) + (q) * 16); \
    }                                                                            \
  }
#define CONV_MM(buf, full)                                                                        \
  {                                                                                               \
    _Pragma("unroll") for (int e = 0; e < 2; ++e) {                                               \
      acc1 = __builtin_amdgcn_mfma_f32_32x32x16_bf16(a[buf][e], b1[buf][e], acc1, 0, 0, 0);       \
      if (full) acc0 = __builtin_amdgcn_mfma_f32_32x32x16_bf16(a[buf][e], b0[buf][e], acc0, 0, 0, 0); \
    }                                                                                             \
  }
    CONV_LD(0, 0, true);
    for (int q = 0; q < 32; q += 2) {
      WAIT_LGKM0(); SB();
      CONV_LD(1, q + 1, true);
      SB();
      CONV_MM(0, true);
      WAIT_LGKM0(); SB();
      if (q + 2 < 32) { CONV_LD(0, q + 2, true); } else { CONV_LD(0, q + 2, false); }
      SB();
      CONV_MM(1, true);
    }
    for (int q = 32; q < 64; q += 2) {
      WAIT_LGKM0(); SB();
      CONV_LD(1, q + 1, false);
      SB();
      CONV_MM(0, false);
      WAIT_LGKM0(); SB();
      if (q + 2 < 64) { CONV_LD(0, q + 2, false); }
      SB();
      CONV_MM(1, false);
    }
    STAMP(3, 2);
    char* yb = smem + wave * HC_CH;
#pragma unroll
    for (int q4 = 0; q4 < 4; ++q4) {
      u32x2 o0 = {pack_bf16(acc0[4 * q4], acc0[4 * q4 + 1]), pack_bf16(acc0[4 * q4 + 2], acc0[4 * q4 + 3])};
      u32x2 o1 = {pack_bf16(acc1[4 * q4], acc1[4 * q4 + 1]), pack_bf16(acc1[4 * q4 + 2], acc1[4 * q4 + 3])};
      *(u32x2*)(yb + (32 * i + 8 * q4 + 4 * hh) * 2) = o0;
      *(u32x2*)(yb + (32 * (32 + i) + 8 * q4 + 4 * hh) * 2) = o1;
    }
  }
  __syncthreads();
  {
    u32x2 dd = *(const u32x2*)(ssmD + d0);
    float yv[4][2];
    int cidx = 255 - (tid >> 2);
    int off = ((cidx & 3) * UB_S + (cidx >> 2)) * 16 + (3 - (tid & 3)) * 4;
#pragma unroll
    for (int ch = 0; ch < 4; ++ch) {
      unsigned c = *(const unsigned*)(smem + ch * HC_CH + tid * 4);
      unsigned kvp = *(const unsigned*)(ubase + ch * UB_CH + off);
      float dv = chsel(dd, ch);
      yv[ch][0] = rbf(bflo(c) + rbf(bfhi(kvp) * dv));
      yv[ch][1] = rbf(bfhi(c) + rbf(bflo(kvp) * dv));
    }
    char* yo = ws + OFF_Y + ((size_t)cg * 2048 + s0) * 8;
    u32x4 o = {pack_bf16(yv[0][0], yv[1][0]), pack_bf16(yv[2][0], yv[3][0]), pack_bf16(yv[0][1], yv[1][1]),
               pack_bf16(yv[2][1], yv[3][1])};
    *(u32x4*)yo = o;
  }
  STAMP(3, 3);
}

DEVI void kvt_part(const u16* __restrict__ kin, const u16* __restrict__ vin, const u16* __restrict__ shiftk,
                   char* __restrict__ ws, int id) {
  const int lane = id & 63, wv = id >> 6;
  const int cgp = wv % 9, tt = wv / 9;
  const int tb = lane >> 3, cc = lane & 7;
  const int s0 = tt * 32 + tb * 4, c0 = cgp * 64 + cc * 8;
  u32x4 kr[7], vr[4], wr[4];
#pragma unroll
  for (int i = 0; i < 7; ++i) {
    int s = s0 - 3 + i;
    s = s < 0 ? 0 : s;
    kr[i] = *(const u32x4*)(kin + (size_t)s * 576 + c0);
  }
#pragma unroll
  for (int i = 0; i < 4; ++i) vr[i] = *(const u32x4*)(vin + (size_t)(s0 + i) * 576 + c0);
#pragma unroll
  for (int i = 0; i < 4; ++i) wr[i] = *(const u32x4*)(shiftk + (size_t)c0 * 4 + i * 8);
  if (s0 == 0) {
    u32x4 z = {0u, 0u, 0u, 0u};
    kr[0] = z; kr[1] = z; kr[2] = z;
  }
#pragma unroll
  for (int ch = 0; ch < 8; ++ch) {
    unsigned w01 = wr[ch >> 1][(ch & 1) * 2], w23 = wr[ch >> 1][(ch & 1) * 2 + 1];
    float w[4] = {bflo(w01), bfhi(w01), bflo(w23), bfhi(w23)};
    float kv[4];
#pragma unroll
    for (int i = 0; i < 4; ++i) {
      float acc = 0.f;
#pragma unroll
      for (int j = 3; j >= 0; --j) {
        unsigned kw = kr[i + 3 - j][ch >> 1];
        acc = rbf(acc + rbf(((ch & 1) ? bfhi(kw) : bflo(kw)) * w[j]));
      }
      unsigned vw = vr[i][ch >> 1];
      kv[i] = acc * ((ch & 1) ? bfhi(vw) : bflo(vw));
    }
    u32x2 o = {pack_bf16(kv[3], kv[2]), pack_bf16(kv[1], kv[0])};
    *(u32x2*)(ws + OFF_KVT + ((size_t)(c0 + ch) * 2048 + 2044 - s0) * 2) = o;
  }
}

constexpr int NT_KVT = 2048 * 576 / 32;
__global__ __launch_bounds__(256) void k_pre(const u16* __restrict__ kin, const u16* __restrict__ vin,
                                             const u16* __restrict__ shiftk, char* __restrict__ ws) {
  kvt_part(kin, vin, shiftk, ws, blockIdx.x * 256 + threadIdx.x);
}

DEVI void layernorm8(const float (&x)[8], float (&o)[8], float2* red, int wave, int t8) {
  float m = 0.f;
#pragma unroll
  for (int j = 0; j < 8; ++j) m += x[j];
  m *= 0.125f;
  float M2 = 0.f;
#pragma unroll
  for (int j = 0; j < 8; ++j) {
    float d = x[j] - m;
    M2 += d * d;
  }
  float n2 = 4.f;
#pragma unroll
  for (int sh = 8; sh <= 32; sh <<= 1) {
    float mo = __shfl_xor(m, sh), M2o = __shfl_xor(M2, sh);
    float d = mo - m;
    M2 = M2 + M2o + d * d * n2;
    m = 0.5f * (m + mo);
    n2 *= 2.f;
  }
  if ((threadIdx.x & 63) < 8) red[wave * 8 + t8] = make_float2(m, M2);
  __syncthreads();
  float2 pr[9];
  float mean = 0.f;
#pragma unroll
  for (int w = 0; w < 9; ++w) {
    pr[w] = red[w * 8 + t8];
    mean += pr[w].x;
  }
  mean *= (1.0f / 9.0f);
  float tq = 0.f;
#pragma unroll
  for (int w = 0; w < 9; ++w) {
    float d = pr[w].x - mean;
    tq += pr[w].y + 64.f * d * d;
  }
  float var = rbf(tq * (1.0f / 576.0f));
  float mb = rbf(mean);
  float sd = rbf(__builtin_amdgcn_sqrtf(rbf(var + 1e-5f)));
  float rs = __builtin_amdgcn_rcpf(sd);
#pragma unroll
  for (int j = 0; j < 8; ++j) o[j] = rbf(rbf(x[j] - mb) * rs);
}

template <int MODE>
__global__ __launch_bounds__(576) void k_row(const u16* __restrict__ q, char* __restrict__ ws, u16* __restrict__ out) {
  __shared__ float2 red[2][72];
  const int tid = threadIdx.x, lane = tid & 63, wave = tid >> 6;
  const int T = blockIdx.x >> 1, half = blockIdx.x & 1;
  const int kp = lane >> 5, G = (lane >> 3) & 3, t8 = lane & 7;
  const int tok = half * 8 + t8, token = T * 16 + tok;
  const int kt = 2 * wave + kp;
  const int chunk = G * 16 + tok;
  const size_t fragoff = ((size_t)(T * 18 + kt) * 64 + chunk) * 16;

  float x[8];
  if (MODE == 0) {
    u32x4 c = *(const u32x4*)(q + (size_t)token * 576 + kt * 32 + G * 8);
    x[0] = bflo(c.x); x[1] = bfhi(c.x); x[2] = bflo(c.y); x[3] = bfhi(c.y);
    x[4] = bflo(c.z); x[5] = bfhi(c.z); x[6] = bflo(c.w); x[7] = bfhi(c.w);
  } else {
    f32x4 pa = {0.f, 0.f, 0.f, 0.f}, pb = {0.f, 0.f, 0.f, 0.f};
#pragma unroll
    for (int sp = 0; sp < 4; ++sp) {
      const char* pp = ws + OFF_P + ((size_t)((sp * 128 + T) * 36 + 2 * kt) * 64 + chunk) * 16;
      pa += *(const f32x4*)pp;
      pb += *(const f32x4*)(pp + 1024);
    }
    u32x4 c = *(const u32x4*)(ws + (MODE == 1 ? OFF_X1 : OFF_X3) + fragoff);
    float xp[8] = {bflo(c.x), bfhi(c.x), bflo(c.y), bfhi(c.y), bflo(c.z), bfhi(c.z), bflo(c.w), bfhi(c.w)};
#pragma unroll
    for (int j = 0; j < 4; ++j) {
      x[j] = rbf(xp[j] + rbf(pa[j]));
      x[4 + j] = rbf(xp[4 + j] + rbf(pb[j]));
    }
  }
  if (MODE == 2) {
    u32x4 o = {pack_bf16(x[0], x[1]), pack_bf16(x[2], x[3]), pack_bf16(x[4], x[5]), pack_bf16(x[6], x[7])};
    *(u32x4*)(out + (size_t)token * 576 + kt * 32 + G * 8) = o;
    return;
  }
  u32x2 ya = *(const u32x2*)(ws + OFF_Y + ((size_t)(8 * kt + 2 * G) * 2048 + token) * 8);
  u32x2 yb = *(const u32x2*)(ws + OFF_Y + ((size_t)(8 * kt + 2 * G + 1) * 2048 + token) * 8);
  float yv[8] = {bflo(ya.x), bfhi(ya.x), bflo(ya.y), bfhi(ya.y), bflo(yb.x), bfhi(yb.x), bflo(yb.y), bfhi(yb.y)};
  float a[8];
  layernorm8(x, a, red[0], wave, t8);
  float xn[8];
#pragma unroll
  for (int j = 0; j < 8; ++j) xn[j] = rbf(x[j] + rbf(a[j] * yv[j]));
  {
    u32x4 o = {pack_bf16(xn[0], xn[1]), pack_bf16(xn[2], xn[3]), pack_bf16(xn[4], xn[5]), pack_bf16(xn[6], xn[7])};
    *(u32x4*)(ws + (MODE == 0 ? OFF_X1 : OFF_X3) + fragoff) = o;
  }
  float b[8];
  layernorm8(xn, b, red[1], wave, t8);
  {
    u32x4 o = {pack_bf16(b[0], b[1]), pack_bf16(b[2], b[3]), pack_bf16(b[4], b[5]), pack_bf16(b[6], b[7])};
    size_t off = ((size_t)(((T >> 4) * 18 + kt) * 16 + (T & 15)) * 64 + chunk) * 16;
    *(u32x4*)(ws + OFF_XN + off) = o;
  }
}

constexpr int STAGE_B = 34 * 1024;
constexpr int NSLOT = 4;
constexpr int GEMM_LDS = NSLOT * STAGE_B;

struct G1State {
  f32x4 acc[9][4];
  bf16x8 xf[2][4];
  bf16x8 wf[9];
};

DEVI void g1_issue(const char* px, const char* pw, char* smem, int slot, int tid, int wave) {
  char* l = smem + slot * STAGE_B;
  glds16(px + tid * 16, l + wave * 1024);
  glds16(px + 8192 + tid * 16, l + 8192 + wave * 1024);
  glds16(pw + tid * 16, l + 16384 + wave * 1024);
  glds16(pw + 8192 + tid * 16, l + 24576 + wave * 1024);
  glds4(pw + 16384 + tid * 4, l + 32768 + wave * 256);
}

template <int PIECE>
DEVI void g1_piece(const char* px, const char* pw, unsigned l, int tid, int wave) {
  if (PIECE == 0) aglds16(px + tid * 16, l + wave * 1024);
  if (PIECE == 1) aglds16(px + 8192 + tid * 16, l + 8192 + wave * 1024);
  if (PIECE == 2) aglds16(pw + tid * 16, l + 16384 + wave * 1024);
  if (PIECE == 3) aglds16(pw + 8192 + tid * 16, l + 24576 + wave * 1024);
  if (PIECE == 4) aglds4(pw + 16384 + tid * 4, l + 32768 + wave * 256);
}
#define G1_MM(f) _Pragma("unroll") for (int t = 0; t < 4; ++t) \
    st.acc[f][t] = __builtin_amdgcn_mfma_f32_16x16x32_bf16(st.wf[f], st.xf[S & 1][t], st.acc[f][t], 0, 0, 0)
template <int S>
DEVI void g1_step(G1State& st, const char* px, const char* pw, char* smem, unsigned lbase, int tid, int wave,
                  const char* lx, const char* lw) {
  constexpr int NS = 18;
  if (S + 1 < NS) {
    wait_vm<(S + 2 < NS) ? 5 : 0>();
    bar();
  }
  SB();
  const char* cw = lw + (S % NSLOT) * STAGE_B;
  const char* gx = px + (S + 3) * 16384;
  const char* gw = pw + (S + 3) * 18432;
  const unsigned l = lbase + ((S + 3) % NSLOT) * STAGE_B;
  constexpr bool ISS = (S + 3 < NS);
  G1_MM(0); st.wf[5] = *(const bf16x8*)(cw + 5 * 1024); if (ISS) g1_piece<0>(gx, gw, l, tid, wave); SB();
  G1_MM(1); st.wf[6] = *(const bf16x8*)(cw + 6 * 1024); if (ISS) g1_piece<1>(gx, gw, l, tid, wave); SB();
  G1_MM(2); st.wf[7] = *(const bf16x8*)(cw + 7 * 1024); if (ISS) g1_piece<2>(gx, gw, l, tid, wave); SB();
  G1_MM(3); st.wf[8] = *(const bf16x8*)(cw + 8 * 1024); if (ISS) g1_piece<3>(gx, gw, l, tid, wave); SB();
  G1_MM(4); if (ISS) g1_piece<4>(gx, gw, l, tid, wave); SB();
  const char* nx = lx + ((S + 1) % NSLOT) * STAGE_B;
  const char* nw = lw + ((S + 1) % NSLOT) * STAGE_B;
  constexpr bool NXT = (S + 1 < NS);
  G1_MM(5);
  if (NXT) {
    st.xf[(S + 1) & 1][0] = *(const bf16x8*)(nx);
    st.xf[(S + 1) & 1][1] = *(const bf16x8*)(nx + 1024);
    st.wf[0] = *(const bf16x8*)(nw);
  }
  SB();
  G1_MM(6);
  if (NXT) {
    st.xf[(S + 1) & 1][2] = *(const bf16x8*)(nx + 2048);
    st.xf[(S + 1) & 1][3] = *(const bf16x8*)(nx + 3072);
    st.wf[1] = *(const bf16x8*)(nw + 1024);
  }
  SB();
  G1_MM(7);
  if (NXT) {
    st.wf[2] = *(const bf16x8*)(nw + 2048);
    st.wf[3] = *(const bf16x8*)(nw + 3072);
    st.wf[4] = *(const bf16x8*)(nw + 4096);
  }
  SB();
  G1_MM(8);
  SB();
  if constexpr (S + 1 < NS) g1_step<S + 1>(st, px, pw, smem, lbase, tid, wave, lx, lw);
}

DEVI float silu_f(float g) { return g * __builtin_amdgcn_rcpf(1.0f + __builtin_amdgcn_exp2f(g * -1.44269504088896f)); }

typedef __attribute__((ext_vector_type(3))) unsigned int u32x3;
template <int BM4>
DEVI void g1_epilogue(const G1State& st, char* hb) {
#pragma unroll
  for (int t = 0; t < 4; ++t) {
    unsigned d[9];
#pragma unroll
    for (int f = 0; f < 9; ++f) {
      f32x2 g = {st.acc[f][t][0], st.acc[f][t][1]};
      f32x2 u = {st.acc[f][t][2], st.acc[f][t][3]};
      f32x2 e = g * -1.44269504088896f;
      e[0] = __builtin_amdgcn_exp2f(e[0]);
      e[1] = __builtin_amdgcn_exp2f(e[1]);
      e = e + 1.0f;
      e[0] = __builtin_amdgcn_rcpf(e[0]);
      e[1] = __builtin_amdgcn_rcpf(e[1]);
      f32x2 h = (g * e) * u;
      d[f] = pack_bf16(h[0], h[1]);
    }
    char* p = hb + (size_t)t * 144 * 1024;
    if (BM4 == 0) {
      *(u32x4*)(p) = u32x4{d[0], d[1], d[2], d[3]};
      *(u32x4*)(p + 1024) = u32x4{d[4], d[5], d[6], d[7]};
      *(unsigned*)(p + 2048) = d[8];
    } else if (BM4 == 1) {
      *(u32x3*)(p + 4) = u32x3{d[0], d[1], d[2]};
      *(u32x4*)(p + 1024) = u32x4{d[3], d[4], d[5], d[6]};
      *(u32x2*)(p + 2048) = u32x2{d[7], d[8]};
    } else if (BM4 == 2) {
      *(u32x2*)(p + 8) = u32x2{d[0], d[1]};
      *(u32x4*)(p + 1024) = u32x4{d[2], d[3], d[4], d[5]};
      *(u32x3*)(p + 2048) = u32x3{d[6], d[7], d[8]};
    } else {
      *(unsigned*)(p + 12) = d[0];
      *(u32x4*)(p + 1024) = u32x4{d[1], d[2], d[3], d[4]};
      *(u32x4*)(p + 2048) = u32x4{d[5], d[6], d[7], d[8]};
    }
  }
}

__global__ __launch_bounds__(512, 2) void k_gemm1(char* __restrict__ ws) {
  extern __shared__ __attribute__((aligned(16))) char smem[];
  const int tid = threadIdx.x, lane = tid & 63;
  const int wave = __builtin_amdgcn_readfirstlane(tid >> 6);
  const int bid = blockIdx.x, xcd = bid & 7, idx = bid >> 3;
  const int mb = (xcd & 1) * 4 + (idx & 3), nb = (xcd >> 1) * 8 + (idx >> 2);
  const int wm = wave & 3, wn = wave >> 2;
  const char* px = ws + OFF_XN + (size_t)mb * 18 * 16384;
  const char* pw = ws + OFF_W13 + (size_t)nb * 18 * 18432;
  const char* lx = smem + wm * 4096 + lane * 16;
  const char* lw = smem + 16384 + wn * 9216 + lane * 16;

  STAMP(1, 0);
  G1State st;
#pragma unroll
  for (int f = 0; f < 9; ++f)
#pragma unroll
    for (int t = 0; t < 4; ++t) st.acc[f][t] = f32x4{0.f, 0.f, 0.f, 0.f};

  const unsigned lbase = lds_addr(smem);
#pragma unroll
  for (int s3 = 0; s3 < 3; ++s3) {
    g1_piece<0>(px + s3 * 16384, pw + s3 * 18432, lbase + s3 * STAGE_B, tid, wave);
    g1_piece<1>(px + s3 * 16384, pw + s3 * 18432, lbase + s3 * STAGE_B, tid, wave);
    g1_piece<2>(px + s3 * 16384, pw + s3 * 18432, lbase + s3 * STAGE_B, tid, wave);
    g1_piece<3>(px + s3 * 16384, pw + s3 * 18432, lbase + s3 * STAGE_B, tid, wave);
    g1_piece<4>(px + s3 * 16384, pw + s3 * 18432, lbase + s3 * STAGE_B, tid, wave);
  }
  wait_vm<10>();
  bar();
  STAMP(1, 1);
#pragma unroll
  for (int t = 0; t < 4; ++t) st.xf[0][t] = *(const bf16x8*)(lx + t * 1024);
#pragma unroll
  for (int f = 0; f < 5; ++f) st.wf[f] = *(const bf16x8*)(lw + f * 1024);
  g1_step<0>(st, px, pw, smem, lbase, tid, wave, lx, lw);

  STAMP(1, 2);
  char* hout = ws + OFF_H;
  {
    const int base = nb * 18 + wn * 9;
    char* hb = hout + ((size_t)((mb * 16 + wm * 4) * 144 + (base >> 2)) * 64 + lane) * 16;
    switch (base & 3) {
      case 0: g1_epilogue<0>(st, hb); break;
      case 1: g1_epilogue<1>(st, hb); break;
      case 2: g1_epilogue<2>(st, hb); break;
      default: g1_epilogue<3>(st, hb); break;
    }
  }
  STAMP(1, 3);
}

constexpr int STAGE2_B = 18 * 1024;
struct G2State {
  f32x4 acc[9][2];
  bf16x8 hf[4][2];
  bf16x8 wf[2][9];
};

DEVI void gload16(bf16x8& d, const char* p) {
  asm volatile("global_load_dwordx4 %0, %1, off" : "=v"(d) : "v"(p) : "memory");
}

template <int R>
DEVI void g2_issue(G2State& st, const char* ph, const char* pw, char* smem, int tid, int wave) {
  char* l = smem + R * STAGE2_B;
  glds16(pw + tid * 16, l + wave * 1024);
  glds16(pw + 8192 + tid * 16, l + 8192 + wave * 1024);
  glds4(pw + 16384 + tid * 4, l + 16384 + wave * 256);
  gload16(st.hf[R][0], ph);
  gload16(st.hf[R][1], ph + 144 * 1024);
}

template <int S>
DEVI void g2_step(G2State& st, const char* ph, const char* pw, char* smem, int tid, int wave, const char* lw) {
  constexpr int NS = 18;
  if (S + 1 < NS) {
    asm volatile("s_waitcnt vmcnt(%2)" : "+v"(st.hf[(S + 1) & 3][0]), "+v"(st.hf[(S + 1) & 3][1]) : "n"((S + 2 < NS) ? 5 : 0) : "memory");
    bar();
  }
  WAIT_LGKM0();
  SB();
  if (S + 1 < NS) {
    const char* cw = lw + ((S + 1) % NSLOT) * STAGE2_B;
#pragma unroll
    for (int f = 0; f < 9; ++f) st.wf[(S + 1) & 1][f] = *(const bf16x8*)(cw + f * 1024);
  }
  if (S + 3 < NS) g2_issue<(S + 3) & 3>(st, ph + (S + 3) * 2048, pw + (S + 3) * 18432, smem, tid, wave);
  SB();
#pragma unroll
  for (int f = 0; f < 9; ++f)
#pragma unroll
    for (int t = 0; t < 2; ++t)
      st.acc[f][t] = __builtin_amdgcn_mfma_f32_16x16x32_bf16(st.wf[S & 1][f], st.hf[S & 3][t], st.acc[f][t], 0, 0, 0);
  SB();
  if constexpr (S + 1 < NS) g2_step<S + 1>(st, ph, pw, smem, tid, wave, lw);
}

__global__ __launch_bounds__(512, 2) void k_gemm2(char* __restrict__ ws) {
  extern __shared__ __attribute__((aligned(16))) char smem[];
  const int tid = threadIdx.x, lane = tid & 63;
  const int wave = __builtin_amdgcn_readfirstlane(tid >> 6);
  const int bid = blockIdx.x, xcd = bid & 7, idx = bid >> 3;
  const int sp = xcd >> 1, mt = (xcd & 1) * 8 + (idx & 7), nb2 = idx >> 3;
  const int kg = wave >> 2, wt = wave & 3;
  const char* ph = ws + OFF_H + ((size_t)((mt * 8 + 2 * wt) * 144 + sp * 36 + kg) * 64 + lane) * 16;
  const char* pw = ws + OFF_W2 + (size_t)(nb2 * 144 + sp * 36) * 9216;
  const char* lw = smem + kg * 9216 + lane * 16;

  STAMP(2, 0);
  G2State st;
#pragma unroll
  for (int f = 0; f < 9; ++f)
#pragma unroll
    for (int t = 0; t < 2; ++t) st.acc[f][t] = f32x4{0.f, 0.f, 0.f, 0.f};

  g2_issue<0>(st, ph, pw, smem, tid, wave);
  g2_issue<1>(st, ph + 2048, pw + 18432, smem, tid, wave);
  g2_issue<2>(st, ph + 4096, pw + 2 * 18432, smem, tid, wave);
  asm volatile("s_waitcnt vmcnt(10)" : "+v"(st.hf[0][0]), "+v"(st.hf[0][1]) :: "memory");
  bar();
  STAMP(2, 1);
#pragma unroll
  for (int f = 0; f < 9; ++f) st.wf[0][f] = *(const bf16x8*)(lw + f * 1024);
  g2_step<0>(st, ph, pw, smem, tid, wave, lw);
  STAMP(2, 2);
  __syncthreads();
  char* xb = smem + (wt * 18) * 1024 + lane * 16;
  if (kg == 1) {
#pragma unroll
    for (int f = 0; f < 9; ++f)
#pragma unroll
      for (int t = 0; t < 2; ++t) *(f32x4*)(xb + (f * 2 + t) * 1024) = st.acc[f][t];
  }
  __syncthreads();
  if (kg == 0) {
    char* pout = ws + OFF_P;
#pragma unroll
    for (int f = 0; f < 9; ++f)
#pragma unroll
      for (int t = 0; t < 2; ++t) {
        f32x4 o = st.acc[f][t] + *(const f32x4*)(xb + (f * 2 + t) * 1024);
        const int T = mt * 8 + wt * 2 + t;
        *(f32x4*)(pout + ((size_t)((sp * 128 + T) * 36 + nb2 * 9 + f) * 64 + lane) * 16) = o;
      }
  }
  STAMP(2, 3);
}

extern "C" void kernel_launch(void* const* d_in, const int* in_sizes, int n_in, void* d_out, int out_size, void* d_ws,
                              size_t ws_size, hipStream_t stream) {
  const u16* q = (const u16*)d_in[0];
  const u16* kin = (const u16*)d_in[1];
  const u16* vin = (const u16*)d_in[2];
  const u16* shiftk = (const u16*)d_in[3];
  const u16* ssmk = (const u16*)d_in[4];
  const u16* ssmD = (const u16*)d_in[5];
  const u16* w1 = (const u16*)d_in[7];
  const u16* w3 = (const u16*)d_in[8];
  const u16* w2 = (const u16*)d_in[9];
  char* ws = (char*)d_ws;
  u16* out = (u16*)d_out;

  hipFuncSetAttribute((const void*)k_conv, hipFuncAttributeMaxDynamicSharedMemorySize, PREP_LDS);
  hipFuncSetAttribute((const void*)k_gemm1, hipFuncAttributeMaxDynamicSharedMemorySize, GEMM_LDS);
  hipFuncSetAttribute((const void*)k_gemm2, hipFuncAttributeMaxDynamicSharedMemorySize, 4 * STAGE2_B);

  k_pre<<<NT_KVT / 256, 256, 0, stream>>>(kin, vin, shiftk, ws);
  k_conv<<<256, 1024, PREP_LDS, stream>>>(ssmk, ssmD, w1, w3, w2, ws);
  k_row<0><<<256, 576, 0, stream>>>(q, ws, out);
  k_gemm1<<<256, 512, GEMM_LDS, stream>>>(ws);
  k_gemm2<<<256, 512, 4 * STAGE2_B, stream>>>(ws);
  k_row<1><<<256, 576, 0, stream>>>(q, ws, out);
  k_gemm1<<<256, 512, GEMM_LDS, stream>>>(ws);
  k_gemm2<<<256, 512, 4 * STAGE2_B, stream>>>(ws);
  k_row<2><<<256, 576, 0, stream>>>(q, ws, out);
}
```

```cpp
#include <hip/hip_runtime.h>
#include <hip/hip_bf16.h>
#include <stdint.h>

typedef __attribute__((ext_vector_type(8))) short bf16x8;
typedef __attribute__((ext_vector_type(4))) float f32x4;
typedef __attribute__((ext_vector_type(16))) float f32x16;
typedef __attribute__((ext_vector_type(4))) unsigned int u32x4;
typedef __attribute__((ext_vector_type(2))) unsigned int u32x2;
typedef __attribute__((ext_vector_type(2))) __bf16 bf16x2_t;
typedef __attribute__((ext_vector_type(2))) float f32x2;
typedef unsigned short u16;

#define AS1 __attribute__((address_space(1)))
#define AS3 __attribute__((address_space(3)))
#define DEVI __device__ __forceinline__

constexpr size_t SZ_W13 = 576ull * 18 * 1024;
constexpr size_t SZ_W2 = 36ull * 144 * 1024;
constexpr size_t SZ_ACT = 2048ull * 576 * 2;
constexpr size_t SZ_H = 128ull * 144 * 1024;
constexpr size_t SZ_P = 4ull * 128 * 36 * 1024;
constexpr size_t OFF_W13 = 0;
constexpr size_t OFF_W2 = OFF_W13 + SZ_W13;
constexpr size_t OFF_Y = OFF_W2 + SZ_W2;
constexpr size_t OFF_X1 = OFF_Y + SZ_ACT;
constexpr size_t OFF_X3 = OFF_X1 + SZ_ACT;
constexpr size_t OFF_XN = OFF_X3 + SZ_ACT;
constexpr size_t OFF_H = OFF_XN + SZ_ACT;
constexpr size_t OFF_P = OFF_H + SZ_H;
constexpr size_t OFF_KVT = OFF_P + SZ_P;

DEVI unsigned pack_bf16(float lo, float hi) {
  f32x2 v = {lo, hi};
  bf16x2_t r = __builtin_convertvector(v, bf16x2_t);
  return __builtin_bit_cast(unsigned, r);
}
DEVI float bflo(unsigned u) { return __uint_as_float(u << 16); }
DEVI float bfhi(unsigned u) { return __uint_as_float(u & 0xffff0000u); }
DEVI float rbf(float f) { return bflo(pack_bf16(f, f)); }

template <int N> DEVI void wait_vm() { asm volatile("s_waitcnt vmcnt(%0)" ::"n"(N) : "memory"); }
DEVI void bar() { __builtin_amdgcn_s_barrier(); }

#define STAMP(kk, id) do {} while (0)

DEVI void glds16(const void* g, void* l) {
  __builtin_amdgcn_global_load_lds((const AS1 void*)g, (AS3 void*)l, 16, 0, 0);
}
DEVI void glds4(const void* g, void* l) {
  __builtin_amdgcn_global_load_lds((const AS1 void*)g, (AS3 void*)l, 4, 0, 0);
}

DEVI void aglds16(const void* g, unsigned lds) {
  unsigned keep;
  asm volatile("s_mov_b32 %0, m0\n\ts_mov_b32 m0, %2\n\ts_nop 0\n\tglobal_load_lds_dwordx4 %1, off\n\ts_mov_b32 m0, %0"
               : "=&s"(keep) : "v"(g), "s"(lds) : "memory");
}
DEVI void aglds4(const void* g, unsigned lds) {
  unsigned keep;
  asm volatile("s_mov_b32 %0, m0\n\ts_mov_b32 m0, %2\n\ts_nop 0\n\tglobal_load_lds_dword %1, off\n\ts_mov_b32 m0, %0"
               : "=&s"(keep) : "v"(g), "s"(lds) : "memory");
}
DEVI unsigned lds_addr(const void* p) { return (unsigned)(size_t)(const AS3 char*)p; }

#define SB() __builtin_amdgcn_sched_barrier(0)
#define WAIT_LGKM0() __builtin_amdgcn_s_waitcnt(0xC07F)
constexpr int HC_STRIDE = 260 * 16;
constexpr int HC_CH = 8 * HC_STRIDE;
constexpr int UB_S = 98;
constexpr int UB_CH = 4 * UB_S * 16;
constexpr int PREP_LDS = 4 * HC_CH + 4 * UB_CH;

DEVI float chsel(u32x2 r, int ch) { return ch == 0 ? bflo(r.x) : ch == 1 ? bfhi(r.x) : ch == 2 ? bflo(r.y) : bfhi(r.y); }

DEVI void transpose8(const u32x4 (&r)[8], u32x4 (&o)[8]) {
#pragma unroll
  for (int c = 0; c < 8; ++c) {
    const unsigned sel = (c & 1) ? 0x07060302u : 0x05040100u;
    unsigned d[4];
#pragma unroll
    for (int i = 0; i < 4; ++i) d[i] = __builtin_amdgcn_perm(r[2 * i + 1][c >> 1], r[2 * i][c >> 1], sel);
    o[c] = u32x4{d[0], d[1], d[2], d[3]};
  }
}

constexpr int RET_FS = 1024 + 16;
constexpr int NU13 = 18 * 9, NU2 = 144;
constexpr int RET_LDS = 64 * RET_FS;
DEVI void retile_unit(const u16* __restrict__ w1, const u16* __restrict__ w3, const u16* __restrict__ w2,
                      char* __restrict__ ws, char* img, int unit, int tid) {
  u32x4 r[8], o[8];
  const int wave = tid >> 6, lane = tid & 63;
  if (unit >= NU13 + NU2) {
    __syncthreads();
  } else if (unit < NU13) {
    const int kt = unit / 9, fgg = unit % 9;
    const int mat = wave & 1, G = (wave >> 1) & 3;
    const int fg = fgg * 64 + lane;
    const u16* src = (mat ? w3 : w1) + (size_t)(kt * 32 + G * 8) * 4608 + fg * 8;
#pragma unroll
    for (int i = 0; i < 8; ++i) r[i] = *(const u32x4*)(src + (size_t)i * 4608);
    transpose8(r, o);
    char* d = img + lane * RET_FS + (G * 16 + mat * 2) * 16;
#pragma unroll
    for (int c = 0; c < 8; ++c) *(u32x4*)(d + (4 * (c >> 1) + (c & 1)) * 16) = o[c];
    __syncthreads();
#pragma unroll
    for (int n = 0; n < 8; ++n) {
      const int fl = wave * 8 + n;
      const int f2 = fgg * 64 + fl;
      const int nb = f2 / 18, ftl = f2 % 18;
      u32x4 v = *(const u32x4*)(img + fl * RET_FS + lane * 16);
      *(u32x4*)(ws + OFF_W13 + ((size_t)((nb * 18 + kt) * 18 + ftl) * 64 + lane) * 16) = v;
    }
  } else {
    const int kt2 = unit - NU13;
    if (tid < 288) {
      const int G = tid / 72, og = tid % 72;
      const u16* src = w2 + (size_t)(kt2 * 32 + 2 * G) * 576 + og * 8;
#pragma unroll
      for (int i = 0; i < 8; ++i) r[i] = *(const u32x4*)(src + (size_t)(8 * (i >> 1) + (i & 1)) * 576);
      transpose8(r, o);
      const int m = og >> 2, g = og & 3;
#pragma unroll
      for (int e = 0; e < 2; ++e) {
        char* d = img + (2 * m + e) * RET_FS + (G * 16 + 4 * g) * 16;
#pragma unroll
        for (int c = 0; c < 4; ++c) *(u32x4*)(d + c * 16) = o[4 * e + c];
      }
    }
    __syncthreads();
    for (int ot = wave; ot < 36; ot += 8) {
      const int nb2 = ot / 9, otl = ot % 9;
      u32x4 v = *(const u32x4*)(img + ot * RET_FS + lane * 16);
      *(u32x4*)(ws + OFF_W2 + ((size_t)((nb2 * 144 + kt2) * 9 + otl) * 64 + lane) * 16) = v;
    }
  }
  __syncthreads();
}

__global__ __launch_bounds__(1024, 4) void k_conv(const u16* __restrict__ ssmk, const u16* __restrict__ ssmD,
                                                 const u16* __restrict__ w1, const u16* __restrict__ w3,
                                                 const u16* __restrict__ w2, char* __restrict__ ws) {
  extern __shared__ __attribute__((aligned(16))) char smem[];
  if (blockIdx.x >= 144) {
    const int hf = threadIdx.x >> 9;
#pragma unroll 1
    for (int it = 0; it < 2; ++it) {
      const int base = (blockIdx.x - 144) * 2 + it * 224;
      if (base >= NU13 + NU2) break;
      retile_unit(w1, w3, w2, ws, smem + hf * RET_LDS, base + hf, threadIdx.x & 511);
    }
    return;
  }
  const int tid = threadIdx.x, lane = tid & 63;
  const int wave = __builtin_amdgcn_readfirstlane(tid >> 6);
  const int cg = blockIdx.x;
  const int d0 = cg * 4;
  const int s0 = tid * 2;
  char* ubase = smem + 4 * HC_CH;

  STAMP(3, 0);
  {
    const int ch = wave & 3, b8 = wave >> 2;
    aglds16(ws + OFF_KVT + (size_t)(d0 + ch) * 4096 + (4 * lane + b8) * 16, lds_addr(ubase) + ch * UB_CH + b8 * (UB_S * 16));
  }
  {
    const int ch = wave & 3, qq = wave >> 2;
    const u16* hsrc = ssmk + (size_t)(d0 + ch) * 2048;
    char* hb = smem + ch * HC_CH;
#pragma unroll
    for (int n2 = 0; n2 < 2; ++n2) {
      int cp = -4 + lane + 64 * (n2 == 0 ? qq : 4);
      bool act = (n2 == 0) || (qq == 0 && cp <= 254);
      if (act) {
        int c0 = cp < 0 ? 0 : cp, c1 = cp + 1 < 0 ? 0 : cp + 1;
        u32x4 lo = *(const u32x4*)(hsrc + 8 * c0);
        u32x4 hi = *(const u32x4*)(hsrc + 8 * c1);
        u32x4 z = {0u, 0u, 0u, 0u};
        if (cp < 0) lo = z;
        if (cp + 1 < 0) hi = z;
        unsigned Dw[8] = {lo.x, lo.y, lo.z, lo.w, hi.x, hi.y, hi.z, hi.w};
        unsigned Ow[7];
#pragma unroll
        for (int t = 0; t < 7; ++t) Ow[t] = (Dw[t] >> 16) | (Dw[t + 1] << 16);
        char* dst = hb + (cp + 4) * 16;
#pragma unroll
        for (int p = 0; p < 8; ++p) {
          int pp = p + 1;
          u32x4 o;
          if (pp & 1) {
            int e = (pp - 1) >> 1;
            o = u32x4{Ow[e], Ow[e + 1], Ow[e + 2], Ow[e + 3]};
          } else {
            int e = pp >> 1;
            o = u32x4{Dw[e], Dw[e + 1], Dw[e + 2], Dw[e + 3]};
          }
          *(u32x4*)(dst + p * HC_STRIDE) = o;
        }
      }
    }
  }
  if (tid < 512) {
    int ch = tid >> 7, rem = tid & 127;
    u32x4 z = {0u, 0u, 0u, 0u};
    *(u32x4*)(ubase + ch * UB_CH + (((rem >> 5) * UB_S) + 64 + (rem & 31)) * 16) = z;
  }
  wait_vm<0>();
  __syncthreads();
  STAMP(3, 1);

  if (wave < 4) {
    f32x16 acc0 = {}, acc1 = {};
    const int i = lane & 31, hh = lane >> 5;
    const char* pa = smem + wave * HC_CH + (lane & 7) * HC_STRIDE + (hh + (i >> 3)) * 16;
    const char* pb1 = ubase + wave * UB_CH + hh * (UB_S * 16) + (31 - i) * 16;
    const char* pb0 = pb1 + 32 * 16;
    bf16x8 a[3][2], b1[3][2], b0[3][2];
#define CONV_LD(buf, q, full)                                                    \
  {                                                                              \
    _Pragma("unroll") for (int e = 0; e < 2; ++e) {                              \
      a[buf][e] = *(const bf16x8*)(pa + (2 * (q) + e) * 32);                     \
      b1[buf][e] = *(const bf16x8*)(pb1 + e * (2 * UB_S * 16) + (q) * 16);       \
      if (full) b0[buf][e] = *(const bf16x8*)(pb0 + e * (2 * UB_S * 16) + (q) * 16); \
    }                                                                            \
  }
#define CONV_MM(buf, full)                                                                        \
  {                                                                                               \
    _Pragma("unroll") for (int e = 0; e < 2; ++e) {                                               \
      acc1 = __builtin_amdgcn_mfma_f32_32x32x16_bf16(a[buf][e], b1[buf][e], acc1, 0, 0, 0);       \
      if (full) acc0 = __builtin_amdgcn_mfma_f32_32x32x16_bf16(a[buf][e], b0[buf][e], acc0, 0, 0, 0); \
    }                                                                                             \
  }
    CONV_LD(0, 0, true);
    CONV_LD(1, 1, true);
    for (int q = 0; q < 30; q += 3) {
      CONV_LD(2, q + 2, true); SB(); CONV_MM(0, true); SB();
      CONV_LD(0, q + 3, true); SB(); CONV_MM(1, true); SB();
      if (q + 4 < 32) { CONV_LD(1, q + 4, true); } else { CONV_LD(1, q + 4, false); }
      SB(); CONV_MM(2, true); SB();
    }
    CONV_LD(2, 32, false); SB(); CONV_MM(0, true); SB();
    CONV_LD(0, 33, false); SB(); CONV_MM(1, true); SB();
    for (int q = 32; q < 62; q += 3) {
      CONV_LD(1, q + 2, false); SB(); CONV_MM(2, false); SB();
      CONV_LD(2, q + 3, false); SB(); CONV_MM(0, false); SB();
      if (q + 4 < 64) { CONV_LD(0, q + 4, false); }
      SB(); CONV_MM(1, false); SB();
    }
    CONV_MM(2, false); SB();
    CONV_MM(0, false); SB();
    STAMP(3, 2);
    char* yb = smem + wave * HC_CH;
#pragma unroll
    for (int q4 = 0; q4 < 4; ++q4) {
      u32x2 o0 = {pack_bf16(acc0[4 * q4], acc0[4 * q4 + 1]), pack_bf16(acc0[4 * q4 + 2], acc0[4 * q4 + 3])};
      u32x2 o1 = {pack_bf16(acc1[4 * q4], acc1[4 * q4 + 1]), pack_bf16(acc1[4 * q4 + 2], acc1[4 * q4 + 3])};
      *(u32x2*)(yb + (32 * i + 8 * q4 + 4 * hh) * 2) = o0;
      *(u32x2*)(yb + (32 * (32 + i) + 8 * q4 + 4 * hh) * 2) = o1;
    }
  }
  __syncthreads();
  {
    u32x2 dd = *(const u32x2*)(ssmD + d0);
    float yv[4][2];
    int cidx = 255 - (tid >> 2);
    int off = ((cidx & 3) * UB_S + (cidx >> 2)) * 16 + (3 - (tid & 3)) * 4;
#pragma unroll
    for (int ch = 0; ch < 4; ++ch) {
      unsigned c = *(const unsigned*)(smem + ch * HC_CH + tid * 4);
      unsigned kvp = *(const unsigned*)(ubase + ch * UB_CH + off);
      float dv = chsel(dd, ch);
      yv[ch][0] = rbf(bflo(c) + rbf(bfhi(kvp) * dv));
      yv[ch][1] = rbf(bfhi(c) + rbf(bflo(kvp) * dv));
    }
    char* yo = ws + OFF_Y + ((size_t)cg * 2048 + s0) * 8;
    u32x4 o = {pack_bf16(yv[0][0], yv[1][0]), pack_bf16(yv[2][0], yv[3][0]), pack_bf16(yv[0][1], yv[1][1]),
               pack_bf16(yv[2][1], yv[3][1])};
    *(u32x4*)yo = o;
  }
  STAMP(3, 3);
}

DEVI void kvt_part(const u16* __restrict__ kin, const u16* __restrict__ vin, const u16* __restrict__ shiftk,
                   char* __restrict__ ws, int id) {
  const int lane = id & 63, wv = id >> 6;
  const int cgp = wv % 9, tt = wv / 9;
  const int tb = lane >> 3, cc = lane & 7;
  const int s0 = tt * 32 + tb * 4, c0 = cgp * 64 + cc * 8;
  u32x4 kr[7], vr[4], wr[4];
#pragma unroll
  for (int i = 0; i < 7; ++i) {
    int s = s0 - 3 + i;
    s = s < 0 ? 0 : s;
    kr[i] = *(const u32x4*)(kin + (size_t)s * 576 + c0);
  }
#pragma unroll
  for (int i = 0; i < 4; ++i) vr[i] = *(const u32x4*)(vin + (size_t)(s0 + i) * 576 + c0);
#pragma unroll
  for (int i = 0; i < 4; ++i) wr[i] = *(const u32x4*)(shiftk + (size_t)c0 * 4 + i * 8);
  if (s0 == 0) {
    u32x4 z = {0u, 0u, 0u, 0u};
    kr[0] = z; kr[1] = z; kr[2] = z;
  }
#pragma unroll
  for (int ch = 0; ch < 8; ++ch) {
    unsigned w01 = wr[ch >> 1][(ch & 1) * 2], w23 = wr[ch >> 1][(ch & 1) * 2 + 1];
    float w[4] = {bflo(w01), bfhi(w01), bflo(w23), bfhi(w23)};
    float kv[4];
#pragma unroll
    for (int i = 0; i < 4; ++i) {
      float acc = 0.f;
#pragma unroll
      for (int j = 3; j >= 0; --j) {
        unsigned kw = kr[i + 3 - j][ch >> 1];
        acc = rbf(acc + rbf(((ch & 1) ? bfhi(kw) : bflo(kw)) * w[j]));
      }
      unsigned vw = vr[i][ch >> 1];
      kv[i] = acc * ((ch & 1) ? bfhi(vw) : bflo(vw));
    }
    u32x2 o = {pack_bf16(kv[3], kv[2]), pack_bf16(kv[1], kv[0])};
    *(u32x2*)(ws + OFF_KVT + ((size_t)(c0 + ch) * 2048 + 2044 - s0) * 2) = o;
  }
}

constexpr int NT_KVT = 2048 * 576 / 32;
__global__ __launch_bounds__(256) void k_pre(const u16* __restrict__ kin, const u16* __restrict__ vin,
                                             const u16* __restrict__ shiftk, char* __restrict__ ws) {
  kvt_part(kin, vin, shiftk, ws, blockIdx.x * 256 + threadIdx.x);
}

DEVI void layernorm8(const float (&x)[8], float (&o)[8], float2* red, int wave, int t8) {
  float m = 0.f;
#pragma unroll
  for (int j = 0; j < 8; ++j) m += x[j];
  m *= 0.125f;
  float M2 = 0.f;
#pragma unroll
  for (int j = 0; j < 8; ++j) {
    float d = x[j] - m;
    M2 += d * d;
  }
  float n2 = 4.f;
#pragma unroll
  for (int sh = 8; sh <= 32; sh <<= 1) {
    float mo = __shfl_xor(m, sh), M2o = __shfl_xor(M2, sh);
    float d = mo - m;
    M2 = M2 + M2o + d * d * n2;
    m = 0.5f * (m + mo);
    n2 *= 2.f;
  }
  if ((threadIdx.x & 63) < 8) red[wave * 8 + t8] = make_float2(m, M2);
  __syncthreads();
  float2 pr[9];
  float mean = 0.f;
#pragma unroll
  for (int w = 0; w < 9; ++w) {
    pr[w] = red[w * 8 + t8];
    mean += pr[w].x;
  }
  mean *= (1.0f / 9.0f);
  float tq = 0.f;
#pragma unroll
  for (int w = 0; w < 9; ++w) {
    float d = pr[w].x - mean;
    tq += pr[w].y + 64.f * d * d;
  }
  float var = rbf(tq * (1.0f / 576.0f));
  float mb = rbf(mean);
  float sd = rbf(__builtin_amdgcn_sqrtf(rbf(var + 1e-5f)));
  float rs = __builtin_amdgcn_rcpf(sd);
#pragma unroll
  for (int j = 0; j < 8; ++j) o[j] = rbf(rbf(x[j] - mb) * rs);
}

template <int MODE>
__global__ __launch_bounds__(576) void k_row(const u16* __restrict__ q, char* __restrict__ ws, u16* __restrict__ out) {
  __shared__ float2 red[2][72];
  const int tid = threadIdx.x, lane = tid & 63, wave = tid >> 6;
  const int T = blockIdx.x >> 1, half = blockIdx.x & 1;
  const int kp = lane >> 5, G = (lane >> 3) & 3, t8 = lane & 7;
  const int tok = half * 8 + t8, token = T * 16 + tok;
  const int kt = 2 * wave + kp;
  const int chunk = G * 16 + tok;
  const size_t fragoff = ((size_t)(T * 18 + kt) * 64 + chunk) * 16;

  float x[8];
  if (MODE == 0) {
    u32x4 c = *(const u32x4*)(q + (size_t)token * 576 + kt * 32 + G * 8);
    x[0] = bflo(c.x); x[1] = bfhi(c.x); x[2] = bflo(c.y); x[3] = bfhi(c.y);
    x[4] = bflo(c.z); x[5] = bfhi(c.z); x[6] = bflo(c.w); x[7] = bfhi(c.w);
  } else {
    f32x4 pa = {0.f, 0.f, 0.f, 0.f}, pb = {0.f, 0.f, 0.f, 0.f};
#pragma unroll
    for (int sp = 0; sp < 4; ++sp) {
      const char* pp = ws + OFF_P + ((size_t)((sp * 128 + T) * 36 + 2 * kt) * 64 + chunk) * 16;
      pa += *(const f32x4*)pp;
      pb += *(const f32x4*)(pp + 1024);
    }
    u32x4 c = *(const u32x4*)(ws + (MODE == 1 ? OFF_X1 : OFF_X3) + fragoff);
    float xp[8] = {bflo(c.x), bfhi(c.x), bflo(c.y), bfhi(c.y), bflo(c.z), bfhi(c.z), bflo(c.w), bfhi(c.w)};
#pragma unroll
    for (int j = 0; j < 4; ++j) {
      x[j] = rbf(xp[j] + rbf(pa[j]));
      x[4 + j] = rbf(xp[4 + j] + rbf(pb[j]));
    }
  }
  if (MODE == 2) {
    u32x4 o = {pack_bf16(x[0], x[1]), pack_bf16(x[2], x[3]), pack_bf16(x[4], x[5]), pack_bf16(x[6], x[7])};
    *(u32x4*)(out + (size_t)token * 576 + kt * 32 + G * 8) = o;
    return;
  }
  u32x2 ya = *(const u32x2*)(ws + OFF_Y + ((size_t)(8 * kt + 2 * G) * 2048 + token) * 8);
  u32x2 yb = *(const u32x2*)(ws + OFF_Y + ((size_t)(8 * kt + 2 * G + 1) * 2048 + token) * 8);
  float yv[8] = {bflo(ya.x), bfhi(ya.x), bflo(ya.y), bfhi(ya.y), bflo(yb.x), bfhi(yb.x), bflo(yb.y), bfhi(yb.y)};
  float a[8];
  layernorm8(x, a, red[0], wave, t8);
  float xn[8];
#pragma unroll
  for (int j = 0; j < 8; ++j) xn[j] = rbf(x[j] + rbf(a[j] * yv[j]));
  {
    u32x4 o = {pack_bf16(xn[0], xn[1]), pack_bf16(xn[2], xn[3]), pack_bf16(xn[4], xn[5]), pack_bf16(xn[6], xn[7])};
    *(u32x4*)(ws + (MODE == 0 ? OFF_X1 : OFF_X3) + fragoff) = o;
  }
  float b[8];
  layernorm8(xn, b, red[1], wave, t8);
  {
    u32x4 o = {pack_bf16(b[0], b[1]), pack_bf16(b[2], b[3]), pack_bf16(b[4], b[5]), pack_bf16(b[6], b[7])};
    size_t off = ((size_t)(((T >> 4) * 18 + kt) * 16 + (T & 15)) * 64 + chunk) * 16;
    *(u32x4*)(ws + OFF_XN + off) = o;
  }
}

constexpr int STAGE_B = 34 * 1024;
constexpr int NSLOT = 4;
constexpr int GEMM_LDS = NSLOT * STAGE_B;

struct G1State {
  f32x4 acc[9][4];
  bf16x8 xf[2][4];
  bf16x8 wf[9];
};

DEVI void g1_issue(const char* px, const char* pw, char* smem, int slot, int tid, int wave) {
  char* l = smem + slot * STAGE_B;
  glds16(px + tid * 16, l + wave * 1024);
  glds16(px + 8192 + tid * 16, l + 8192 + wave * 1024);
  glds16(pw + tid * 16, l + 16384 + wave * 1024);
  glds16(pw + 8192 + tid * 16, l + 24576 + wave * 1024);
  glds4(pw + 16384 + tid * 4, l + 32768 + wave * 256);
}

template <int PIECE>
DEVI void g1_piece(const char* px, const char* pw, unsigned l, int tid, int wave) {
  if (PIECE == 0) aglds16(px + tid * 16, l + wave * 1024);
  if (PIECE == 1) aglds16(px + 8192 + tid * 16, l + 8192 + wave * 1024);
  if (PIECE == 2) aglds16(pw + tid * 16, l + 16384 + wave * 1024);
  if (PIECE == 3) aglds16(pw + 8192 + tid * 16, l + 24576 + wave * 1024);
  if (PIECE == 4) aglds4(pw + 16384 + tid * 4, l + 32768 + wave * 256);
}
#define G1_MM(f) _Pragma("unroll") for (int t = 0; t < 4; ++t) \
    st.acc[f][t] = __builtin_amdgcn_mfma_f32_16x16x32_bf16(st.wf[f], st.xf[S & 1][t], st.acc[f][t], 0, 0, 0)
template <int S>
DEVI void g1_step(G1State& st, const char* px, const char* pw, char* smem, unsigned lbase, int tid, int wave,
                  const char* lx, const char* lw) {
  constexpr int NS = 18;
  if (S + 1 < NS) {
    wait_vm<(S + 2 < NS) ? 5 : 0>();
    bar();
  }
  SB();
  const char* cw = lw + (S % NSLOT) * STAGE_B;
  const char* gx = px + (S + 3) * 16384;
  const char* gw = pw + (S + 3) * 18432;
  const unsigned l = lbase + ((S + 3) % NSLOT) * STAGE_B;
  constexpr bool ISS = (S + 3 < NS);
  G1_MM(0); st.wf[5] = *(const bf16x8*)(cw + 5 * 1024); if (ISS) g1_piece<0>(gx, gw, l, tid, wave); SB();
  G1_MM(1); st.wf[6] = *(const bf16x8*)(cw + 6 * 1024); if (ISS) g1_piece<1>(gx, gw, l, tid, wave); SB();
  G1_MM(2); st.wf[7] = *(const bf16x8*)(cw + 7 * 1024); if (ISS) g1_piece<2>(gx, gw, l, tid, wave); SB();
  G1_MM(3); st.wf[8] = *(const bf16x8*)(cw + 8 * 1024); if (ISS) g1_piece<3>(gx, gw, l, tid, wave); SB();
  G1_MM(4); if (ISS) g1_piece<4>(gx, gw, l, tid, wave); SB();
  const char* nx = lx + ((S + 1) % NSLOT) * STAGE_B;
  const char* nw = lw + ((S + 1) % NSLOT) * STAGE_B;
  constexpr bool NXT = (S + 1 < NS);
  G1_MM(5);
  if (NXT) {
    st.xf[(S + 1) & 1][0] = *(const bf16x8*)(nx);
    st.xf[(S + 1) & 1][1] = *(const bf16x8*)(nx + 1024);
    st.wf[0] = *(const bf16x8*)(nw);
  }
  SB();
  G1_MM(6);
  if (NXT) {
    st.xf[(S + 1) & 1][2] = *(const bf16x8*)(nx + 2048);
    st.xf[(S + 1) & 1][3] = *(const bf16x8*)(nx + 3072);
    st.wf[1] = *(const bf16x8*)(nw + 1024);
  }
  SB();
  G1_MM(7);
  if (NXT) {
    st.wf[2] = *(const bf16x8*)(nw + 2048);
    st.wf[3] = *(const bf16x8*)(nw + 3072);
    st.wf[4] = *(const bf16x8*)(nw + 4096);
  }
  SB();
  G1_MM(8);
  SB();
  if constexpr (S + 1 < NS) g1_step<S + 1>(st, px, pw, smem, lbase, tid, wave, lx, lw);
}

DEVI float silu_f(float g) { return g * __builtin_amdgcn_rcpf(1.0f + __builtin_amdgcn_exp2f(g * -1.44269504088896f)); }

typedef __attribute__((ext_vector_type(3))) unsigned int u32x3;
template <int BM4>
DEVI void g1_epilogue(const G1State& st, char* hb) {
#pragma unroll
  for (int t = 0; t < 4; ++t) {
    unsigned d[9];
#pragma unroll
    for (int f = 0; f < 9; ++f) {
      f32x2 g = {st.acc[f][t][0], st.acc[f][t][1]};
      f32x2 u = {st.acc[f][t][2], st.acc[f][t][3]};
      f32x2 e = g * -1.44269504088896f;
      e[0] = __builtin_amdgcn_exp2f(e[0]);
      e[1] = __builtin_amdgcn_exp2f(e[1]);
      e = e + 1.0f;
      e[0] = __builtin_amdgcn_rcpf(e[0]);
      e[1] = __builtin_amdgcn_rcpf(e[1]);
      f32x2 h = (g * e) * u;
      d[f] = pack_bf16(h[0], h[1]);
    }
    char* p = hb + (size_t)t * 144 * 1024;
    if (BM4 == 0) {
      *(u32x4*)(p) = u32x4{d[0], d[1], d[2], d[3]};
      *(u32x4*)(p + 1024) = u32x4{d[4], d[5], d[6], d[7]};
      *(unsigned*)(p + 2048) = d[8];
    } else if (BM4 == 1) {
      *(u32x3*)(p + 4) = u32x3{d[0], d[1], d[2]};
      *(u32x4*)(p + 1024) = u32x4{d[3], d[4], d[5], d[6]};
      *(u32x2*)(p + 2048) = u32x2{d[7], d[8]};
    } else if (BM4 == 2) {
      *(u32x2*)(p + 8) = u32x2{d[0], d[1]};
      *(u32x4*)(p + 1024) = u32x4{d[2], d[3], d[4], d[5]};
      *(u32x3*)(p + 2048) = u32x3{d[6], d[7], d[8]};
    } else {
      *(unsigned*)(p + 12) = d[0];
      *(u32x4*)(p + 1024) = u32x4{d[1], d[2], d[3], d[4]};
      *(u32x4*)(p + 2048) = u32x4{d[5], d[6], d[7], d[8]};
    }
  }
}

__global__ __launch_bounds__(512, 2) void k_gemm1(char* __restrict__ ws) {
  extern __shared__ __attribute__((aligned(16))) char smem[];
  const int tid = threadIdx.x, lane = tid & 63;
  const int wave = __builtin_amdgcn_readfirstlane(tid >> 6);
  const int bid = blockIdx.x, xcd = bid & 7, idx = bid >> 3;
  const int mb = (xcd & 1) * 4 + (idx & 3), nb = (xcd >> 1) * 8 + (idx >> 2);
  const int wm = wave & 3, wn = wave >> 2;
  const char* px = ws + OFF_XN + (size_t)mb * 18 * 16384;
  const char* pw = ws + OFF_W13 + (size_t)nb * 18 * 18432;
  const char* lx = smem + wm * 4096 + lane * 16;
  const char* lw = smem + 16384 + wn * 9216 + lane * 16;

  STAMP(1, 0);
  G1State st;
#pragma unroll
  for (int f = 0; f < 9; ++f)
#pragma unroll
    for (int t = 0; t < 4; ++t) st.acc[f][t] = f32x4{0.f, 0.f, 0.f, 0.f};

  const unsigned lbase = lds_addr(smem);
#pragma unroll
  for (int s3 = 0; s3 < 3; ++s3) {
    g1_piece<0>(px + s3 * 16384, pw + s3 * 18432, lbase + s3 * STAGE_B, tid, wave);
    g1_piece<1>(px + s3 * 16384, pw + s3 * 18432, lbase + s3 * STAGE_B, tid, wave);
    g1_piece<2>(px + s3 * 16384, pw + s3 * 18432, lbase + s3 * STAGE_B, tid, wave);
    g1_piece<3>(px + s3 * 16384, pw + s3 * 18432, lbase + s3 * STAGE_B, tid, wave);
    g1_piece<4>(px + s3 * 16384, pw + s3 * 18432, lbase + s3 * STAGE_B, tid, wave);
  }
  wait_vm<10>();
  bar();
  STAMP(1, 1);
#pragma unroll
  for (int t = 0; t < 4; ++t) st.xf[0][t] = *(const bf16x8*)(lx + t * 1024);
#pragma unroll
  for (int f = 0; f < 5; ++f) st.wf[f] = *(const bf16x8*)(lw + f * 1024);
  g1_step<0>(st, px, pw, smem, lbase, tid, wave, lx, lw);

  STAMP(1, 2);
  char* hout = ws + OFF_H;
  {
    const int base = nb * 18 + wn * 9;
    char* hb = hout + ((size_t)((mb * 16 + wm * 4) * 144 + (base >> 2)) * 64 + lane) * 16;
    switch (base & 3) {
      case 0: g1_epilogue<0>(st, hb); break;
      case 1: g1_epilogue<1>(st, hb); break;
      case 2: g1_epilogue<2>(st, hb); break;
      default: g1_epilogue<3>(st, hb); break;
    }
  }
  STAMP(1, 3);
}

constexpr int STAGE2_B = 18 * 1024;
struct G2State {
  f32x4 acc[9][2];
  bf16x8 hf[4][2];
  bf16x8 wf[2][9];
};

DEVI void gload16(bf16x8& d, const char* p) {
  asm volatile("global_load_dwordx4 %0, %1, off" : "=v"(d) : "v"(p) : "memory");
}

template <int R, int PIECE>
DEVI void g2_piece(G2State& st, const char* ph, const char* pw, unsigned lbase, int tid, int wave) {
  const unsigned l = lbase + R * STAGE2_B;
  if (PIECE == 0) aglds16(pw + tid * 16, l + wave * 1024);
  if (PIECE == 1) aglds16(pw + 8192 + tid * 16, l + 8192 + wave * 1024);
  if (PIECE == 2) aglds4(pw + 16384 + tid * 4, l + 16384 + wave * 256);
  if (PIECE == 3) gload16(st.hf[R][0], ph);
  if (PIECE == 4) gload16(st.hf[R][1], ph + 144 * 1024);
}
#define G2_MM(f) _Pragma("unroll") for (int t = 0; t < 2; ++t) \
    st.acc[f][t] = __builtin_amdgcn_mfma_f32_16x16x32_bf16(st.wf[S & 1][f], st.hf[S & 3][t], st.acc[f][t], 0, 0, 0)
template <int S>
DEVI void g2_step(G2State& st, const char* ph, const char* pw, unsigned lbase, int tid, int wave, const char* lw) {
  constexpr int NS = 18;
  if (S + 1 < NS) {
    asm volatile("s_waitcnt vmcnt(%2)" : "+v"(st.hf[(S + 1) & 3][0]), "+v"(st.hf[(S + 1) & 3][1]) : "n"((S + 2 < NS) ? 5 : 0) : "memory");
    bar();
  }
  SB();
  constexpr bool NXT = (S + 1 < NS), ISS = (S + 3 < NS);
  constexpr int R = (S + 3) & 3;
  const char* cw = lw + ((S + 1) % NSLOT) * STAGE2_B;
  const char* gh = ph + (S + 3) * 2048;
  const char* gw = pw + (S + 3) * 18432;
  G2_MM(0); if (NXT) { st.wf[(S + 1) & 1][0] = *(const bf16x8*)(cw); st.wf[(S + 1) & 1][1] = *(const bf16x8*)(cw + 1024); } if (ISS) g2_piece<R, 0>(st, gh, gw, lbase, tid, wave); SB();
  G2_MM(1); if (NXT) { st.wf[(S + 1) & 1][2] = *(const bf16x8*)(cw + 2048); st.wf[(S + 1) & 1][3] = *(const bf16x8*)(cw + 3072); } if (ISS) g2_piece<R, 1>(st, gh, gw, lbase, tid, wave); SB();
  G2_MM(2); if (NXT) { st.wf[(S + 1) & 1][4] = *(const bf16x8*)(cw + 4096); st.wf[(S + 1) & 1][5] = *(const bf16x8*)(cw + 5120); } if (ISS) g2_piece<R, 2>(st, gh, gw, lbase, tid, wave); SB();
  G2_MM(3); if (NXT) { st.wf[(S + 1) & 1][6] = *(const bf16x8*)(cw + 6144); st.wf[(S + 1) & 1][7] = *(const bf16x8*)(cw + 7168); } if (ISS) g2_piece<R, 3>(st, gh, gw, lbase, tid, wave); SB();
  G2_MM(4); if (NXT) { st.wf[(S + 1) & 1][8] = *(const bf16x8*)(cw + 8192); } if (ISS) g2_piece<R, 4>(st, gh, gw, lbase, tid, wave); SB();
  G2_MM(5); G2_MM(6); G2_MM(7); G2_MM(8);
  SB();
  if constexpr (S + 1 < NS) g2_step<S + 1>(st, ph, pw, lbase, tid, wave, lw);
}

__global__ __launch_bounds__(512, 2) void k_gemm2(char* __restrict__ ws) {
  extern __shared__ __attribute__((aligned(16))) char smem[];
  const int tid = threadIdx.x, lane = tid & 63;
  const int wave = __builtin_amdgcn_readfirstlane(tid >> 6);
  const int bid = blockIdx.x, xcd = bid & 7, idx = bid >> 3;
  const int sp = xcd >> 1, mt = (xcd & 1) * 8 + (idx & 7), nb2 = idx >> 3;
  const int kg = wave >> 2, wt = wave & 3;
  const char* ph = ws + OFF_H + ((size_t)((mt * 8 + 2 * wt) * 144 + sp * 36 + kg) * 64 + lane) * 16;
  const char* pw = ws + OFF_W2 + (size_t)(nb2 * 144 + sp * 36) * 9216;
  const char* lw = smem + kg * 9216 + lane * 16;

  STAMP(2, 0);
  G2State st;
#pragma unroll
  for (int f = 0; f < 9; ++f)
#pragma unroll
    for (int t = 0; t < 2; ++t) st.acc[f][t] = f32x4{0.f, 0.f, 0.f, 0.f};

  const unsigned lbase = lds_addr(smem);
  g2_piece<0, 0>(st, ph, pw, lbase, tid, wave); g2_piece<0, 1>(st, ph, pw, lbase, tid, wave); g2_piece<0, 2>(st, ph, pw, lbase, tid, wave);
  g2_piece<0, 3>(st, ph, pw, lbase, tid, wave); g2_piece<0, 4>(st, ph, pw, lbase, tid, wave);
  g2_piece<1, 0>(st, ph + 2048, pw + 18432, lbase, tid, wave); g2_piece<1, 1>(st, ph + 2048, pw + 18432, lbase, tid, wave); g2_piece<1, 2>(st, ph + 2048, pw + 18432, lbase, tid, wave);
  g2_piece<1, 3>(st, ph + 2048, pw + 18432, lbase, tid, wave); g2_piece<1, 4>(st, ph + 2048, pw + 18432, lbase, tid, wave);
  g2_piece<2, 0>(st, ph + 4096, pw + 2 * 18432, lbase, tid, wave); g2_piece<2, 1>(st, ph + 4096, pw + 2 * 18432, lbase, tid, wave); g2_piece<2, 2>(st, ph + 4096, pw + 2 * 18432, lbase, tid, wave);
  g2_piece<2, 3>(st, ph + 4096, pw + 2 * 18432, lbase, tid, wave); g2_piece<2, 4>(st, ph + 4096, pw + 2 * 18432, lbase, tid, wave);
  asm volatile("s_waitcnt vmcnt(10)" : "+v"(st.hf[0][0]), "+v"(st.hf[0][1]) :: "memory");
  bar();
  STAMP(2, 1);
#pragma unroll
  for (int f = 0; f < 9; ++f) st.wf[0][f] = *(const bf16x8*)(lw + f * 1024);
  g2_step<0>(st, ph, pw, lbase, tid, wave, lw);
  STAMP(2, 2);
  __syncthreads();
  char* xb = smem + (wt * 18) * 1024 + lane * 16;
  if (kg == 1) {
#pragma unroll
    for (int f = 0; f < 5; ++f)
#pragma unroll
      for (int t = 0; t < 2; ++t) *(f32x4*)(xb + (f * 2 + t) * 1024) = st.acc[f][t];
  } else {
#pragma unroll
    for (int f = 5; f < 9; ++f)
#pragma unroll
      for (int t = 0; t < 2; ++t) *(f32x4*)(xb + (f * 2 + t) * 1024) = st.acc[f][t];
  }
  __syncthreads();
  {
    char* pout = ws + OFF_P;
#pragma unroll
    for (int f = 0; f < 9; ++f) {
      if ((f < 5) == (kg == 0)) {
#pragma unroll
        for (int t = 0; t < 2; ++t) {
          f32x4 o = st.acc[f][t] + *(const f32x4*)(xb + (f * 2 + t) * 1024);
          const int T = mt * 8 + wt * 2 + t;
          *(f32x4*)(pout + ((size_t)((sp * 128 + T) * 36 + nb2 * 9 + f) * 64 + lane) * 16) = o;
        }
      }
    }
  }
  STAMP(2, 3);
}

extern "C" void kernel_launch(void* const* d_in, const int* in_sizes, int n_in, void* d_out, int out_size, void* d_ws,
                              size_t ws_size, hipStream_t stream) {
  const u16* q = (const u16*)d_in[0];
  const u16* kin = (const u16*)d_in[1];
  const u16* vin = (const u16*)d_in[2];
  const u16* shiftk = (const u16*)d_in[3];
  const u16* ssmk = (const u16*)d_in[4];
  const u16* ssmD = (const u16*)d_in[5];
  const u16* w1 = (const u16*)d_in[7];
  const u16* w3 = (const u16*)d_in[8];
  const u16* w2 = (const u16*)d_in[9];
  char* ws = (char*)d_ws;
  u16* out = (u16*)d_out;

  hipFuncSetAttribute((const void*)k_conv, hipFuncAttributeMaxDynamicSharedMemorySize, PREP_LDS);
  hipFuncSetAttribute((const void*)k_gemm1, hipFuncAttributeMaxDynamicSharedMemorySize, GEMM_LDS);
  hipFuncSetAttribute((const void*)k_gemm2, hipFuncAttributeMaxDynamicSharedMemorySize, 4 * STAGE2_B);

  k_pre<<<NT_KVT / 256, 256, 0, stream>>>(kin, vin, shiftk, ws);
  k_conv<<<256, 1024, PREP_LDS, stream>>>(ssmk, ssmD, w1, w3, w2, ws);
  k_row<0><<<256, 576, 0, stream>>>(q, ws, out);
  k_gemm1<<<256, 512, GEMM_LDS, stream>>>(ws);
  k_gemm2<<<256, 512, 4 * STAGE2_B, stream>>>(ws);
  k_row<1><<<256, 576, 0, stream>>>(q, ws, out);
  k_gemm1<<<256, 512, GEMM_LDS, stream>>>(ws);
  k_gemm2<<<256, 512, 4 * STAGE2_B, stream>>>(ws);
  k_row<2><<<256, 576, 0, stream>>>(q, ws, out);
}
```

```cpp
#include <hip/hip_runtime.h>
#include <hip/hip_bf16.h>
#include <stdint.h>

typedef __attribute__((ext_vector_type(8))) short bf16x8;
typedef __attribute__((ext_vector_type(4))) float f32x4;
typedef __attribute__((ext_vector_type(16))) float f32x16;
typedef __attribute__((ext_vector_type(4))) unsigned int u32x4;
typedef __attribute__((ext_vector_type(2))) unsigned int u32x2;
typedef __attribute__((ext_vector_type(2))) __bf16 bf16x2_t;
typedef __attribute__((ext_vector_type(2))) float f32x2;
typedef unsigned short u16;

#define AS1 __attribute__((address_space(1)))
#define AS3 __attribute__((address_space(3)))
#define DEVI __device__ __forceinline__

constexpr size_t SZ_W13 = 576ull * 18 * 1024;
constexpr size_t SZ_W2 = 36ull * 144 * 1024;
constexpr size_t SZ_ACT = 2048ull * 576 * 2;
constexpr size_t SZ_H = 128ull * 144 * 1024;
constexpr size_t SZ_P = 4ull * 128 * 36 * 1024;
constexpr size_t OFF_W13 = 0;
constexpr size_t OFF_W2 = OFF_W13 + SZ_W13;
constexpr size_t OFF_Y = OFF_W2 + SZ_W2;
constexpr size_t OFF_X1 = OFF_Y + SZ_ACT;
constexpr size_t OFF_X3 = OFF_X1 + SZ_ACT;
constexpr size_t OFF_XN = OFF_X3 + SZ_ACT;
constexpr size_t OFF_H = OFF_XN + SZ_ACT;
constexpr size_t OFF_P = OFF_H + SZ_H;
constexpr size_t OFF_KVT = OFF_P + SZ_P;

DEVI unsigned pack_bf16(float lo, float hi) {
  f32x2 v = {lo, hi};
  bf16x2_t r = __builtin_convertvector(v, bf16x2_t);
  return __builtin_bit_cast(unsigned, r);
}
DEVI float bflo(unsigned u) { return __uint_as_float(u << 16); }
DEVI float bfhi(unsigned u) { return __uint_as_float(u & 0xffff0000u); }
DEVI float rbf(float f) { return bflo(pack_bf16(f, f)); }

template <int N> DEVI void wait_vm() { asm volatile("s_waitcnt vmcnt(%0)" ::"n"(N) : "memory"); }
DEVI void bar() { __builtin_amdgcn_s_barrier(); }

#define STAMP(kk, id) do {} while (0)

DEVI void glds16(const void* g, void* l) {
  __builtin_amdgcn_global_load_lds((const AS1 void*)g, (AS3 void*)l, 16, 0, 0);
}
DEVI void glds4(const void* g, void* l) {
  __builtin_amdgcn_global_load_lds((const AS1 void*)g, (AS3 void*)l, 4, 0, 0);
}

DEVI void aglds16(const void* g, unsigned lds) {
  unsigned keep;
  asm volatile("s_mov_b32 %0, m0\n\ts_mov_b32 m0, %2\n\ts_nop 0\n\tglobal_load_lds_dwordx4 %1, off\n\ts_mov_b32 m0, %0"
               : "=&s"(keep) : "v"(g), "s"(lds) : "memory");
}
DEVI void aglds4(const void* g, unsigned lds) {
  unsigned keep;
  asm volatile("s_mov_b32 %0, m0\n\ts_mov_b32 m0, %2\n\ts_nop 0\n\tglobal_load_lds_dword %1, off\n\ts_mov_b32 m0, %0"
               : "=&s"(keep) : "v"(g), "s"(lds) : "memory");
}
DEVI unsigned lds_addr(const void* p) { return (unsigned)(size_t)(const AS3 char*)p; }

#define SB() __builtin_amdgcn_sched_barrier(0)
#define WAIT_LGKM0() __builtin_amdgcn_s_waitcnt(0xC07F)
constexpr int HC_STRIDE = 260 * 16;
constexpr int HC_CH = 8 * HC_STRIDE;
constexpr int UB_S = 98;
constexpr int UB_CH = 4 * UB_S * 16;
constexpr int PREP_LDS = 4 * HC_CH + 4 * UB_CH;

DEVI float chsel(u32x2 r, int ch) { return ch == 0 ? bflo(r.x) : ch == 1 ? bfhi(r.x) : ch == 2 ? bflo(r.y) : bfhi(r.y); }

DEVI void transpose8(const u32x4 (&r)[8], u32x4 (&o)[8]) {
#pragma unroll
  for (int c = 0; c < 8; ++c) {
    const unsigned sel = (c & 1) ? 0x07060302u : 0x05040100u;
    unsigned d[4];
#pragma unroll
    for (int i = 0; i < 4; ++i) d[i] = __builtin_amdgcn_perm(r[2 * i + 1][c >> 1], r[2 * i][c >> 1], sel);
    o[c] = u32x4{d[0], d[1], d[2], d[3]};
  }
}

constexpr int RET_FS = 1024 + 16;
constexpr int NU13 = 18 * 9, NU2 = 144;
constexpr int RET_LDS = 64 * RET_FS;
DEVI void retile_unit(const u16* __restrict__ w1, const u16* __restrict__ w3, const u16* __restrict__ w2,
                      char* __restrict__ ws, char* img, int unit, int tid) {
  u32x4 r[8], o[8];
  const int wave = tid >> 6, lane = tid & 63;
  if (unit >= NU13 + NU2) {
    __syncthreads();
  } else if (unit < NU13) {
    const int kt = unit / 9, fgg = unit % 9;
    const int mat = wave & 1, G = (wave >> 1) & 3;
    const int fg = fgg * 64 + lane;
    const u16* src = (mat ? w3 : w1) + (size_t)(kt * 32 + G * 8) * 4608 + fg * 8;
#pragma unroll
    for (int i = 0; i < 8; ++i) r[i] = *(const u32x4*)(src + (size_t)i * 4608);
    transpose8(r, o);
    char* d = img + lane * RET_FS + (G * 16 + mat * 2) * 16;
#pragma unroll
    for (int c = 0; c < 8; ++c) *(u32x4*)(d + (4 * (c >> 1) + (c & 1)) * 16) = o[c];
    __syncthreads();
#pragma unroll
    for (int n = 0; n < 8; ++n) {
      const int fl = wave * 8 + n;
      const int f2 = fgg * 64 + fl;
      const int nb = f2 / 18, ftl = f2 % 18;
      u32x4 v = *(const u32x4*)(img + fl * RET_FS + lane * 16);
      *(u32x4*)(ws + OFF_W13 + ((size_t)((nb * 18 + kt) * 18 + ftl) * 64 + lane) * 16) = v;
    }
  } else {
    const int kt2 = unit - NU13;
    if (tid < 288) {
      const int G = tid / 72, og = tid % 72;
      const u16* src = w2 + (size_t)(kt2 * 32 + 2 * G) * 576 + og * 8;
#pragma unroll
      for (int i = 0; i < 8; ++i) r[i] = *(const u32x4*)(src + (size_t)(8 * (i >> 1) + (i & 1)) * 576);
      transpose8(r, o);
      const int m = og >> 2, g = og & 3;
#pragma unroll
      for (int e = 0; e < 2; ++e) {
        char* d = img + (2 * m + e) * RET_FS + (G * 16 + 4 * g) * 16;
#pragma unroll
        for (int c = 0; c < 4; ++c) *(u32x4*)(d + c * 16) = o[4 * e + c];
      }
    }
    __syncthreads();
    for (int ot = wave; ot < 36; ot += 8) {
      const int nb2 = ot / 9, otl = ot % 9;
      u32x4 v = *(const u32x4*)(img + ot * RET_FS + lane * 16);
      *(u32x4*)(ws + OFF_W2 + ((size_t)((nb2 * 144 + kt2) * 9 + otl) * 64 + lane) * 16) = v;
    }
  }
  __syncthreads();
}

__global__ __launch_bounds__(1024, 4) void k_conv(const u16* __restrict__ ssmk, const u16* __restrict__ ssmD,
                                                 const u16* __restrict__ w1, const u16* __restrict__ w3,
                                                 const u16* __restrict__ w2, char* __restrict__ ws) {
  extern __shared__ __attribute__((aligned(16))) char smem[];
  if (blockIdx.x >= 144) {
    const int hf = threadIdx.x >> 9;
#pragma unroll 1
    for (int it = 0; it < 2; ++it) {
      const int base = (blockIdx.x - 144) * 2 + it * 224;
      if (base >= NU13 + NU2) break;
      retile_unit(w1, w3, w2, ws, smem + hf * RET_LDS, base + hf, threadIdx.x & 511);
    }
    return;
  }
  const int tid = threadIdx.x, lane = tid & 63;
  const int wave = __builtin_amdgcn_readfirstlane(tid >> 6);
  const int cg = blockIdx.x;
  const int d0 = cg * 4;
  const int s0 = tid * 2;
  char* ubase = smem + 4 * HC_CH;

  STAMP(3, 0);
  {
    const int ch = wave & 3, b8 = wave >> 2;
    aglds16(ws + OFF_KVT + (size_t)(d0 + ch) * 4096 + (4 * lane + b8) * 16, lds_addr(ubase) + ch * UB_CH + b8 * (UB_S * 16));
  }
  {
    const int ch = wave & 3, qq = wave >> 2;
    const u16* hsrc = ssmk + (size_t)(d0 + ch) * 2048;
    char* hb = smem + ch * HC_CH;
#pragma unroll
    for (int n2 = 0; n2 < 2; ++n2) {
      int cp = -4 + lane + 64 * (n2 == 0 ? qq : 4);
      bool act = (n2 == 0) || (qq == 0 && cp <= 254);
      if (act) {
        int c0 = cp < 0 ? 0 : cp, c1 = cp + 1 < 0 ? 0 : cp + 1;
        u32x4 lo = *(const u32x4*)(hsrc + 8 * c0);
        u32x4 hi = *(const u32x4*)(hsrc + 8 * c1);
        u32x4 z = {0u, 0u, 0u, 0u};
        if (cp < 0) lo = z;
        if (cp + 1 < 0) hi = z;
        unsigned Dw[8] = {lo.x, lo.y, lo.z, lo.w, hi.x, hi.y, hi.z, hi.w};
        unsigned Ow[7];
#pragma unroll
        for (int t = 0; t < 7; ++t) Ow[t] = (Dw[t] >> 16) | (Dw[t + 1] << 16);
        char* dst = hb + (cp + 4) * 16;
#pragma unroll
        for (int p = 0; p < 8; ++p) {
          int pp = p + 1;
          u32x4 o;
          if (pp & 1) {
            int e = (pp - 1) >> 1;
            o = u32x4{Ow[e], Ow[e + 1], Ow[e + 2], Ow[e + 3]};
          } else {
            int e = pp >> 1;
            o = u32x4{Dw[e], Dw[e + 1], Dw[e + 2], Dw[e + 3]};
          }
          *(u32x4*)(dst + p * HC_STRIDE) = o;
        }
      }
    }
  }
  if (tid < 512) {
    int ch = tid >> 7, rem = tid & 127;
    u32x4 z = {0u, 0u, 0u, 0u};
    *(u32x4*)(ubase + ch * UB_CH + (((rem >> 5) * UB_S) + 64 + (rem & 31)) * 16) = z;
  }
  wait_vm<0>();
  __syncthreads();
  STAMP(3, 1);

  if (wave < 4) {
    f32x16 acc0 = {}, acc1 = {};
    const int i = lane & 31, hh = lane >> 5;
    const char* pa = smem + wave * HC_CH + (lane & 7) * HC_STRIDE + (hh + (i >> 3)) * 16;
    const char* pb1 = ubase + wave * UB_CH + hh * (UB_S * 16) + (31 - i) * 16;
    const char* pb0 = pb1 + 32 * 16;
    bf16x8 a[3][2], b1[3][2], b0[3][2];
#define CONV_LD(buf, q, full)                                                    \
  {                                                                              \
    _Pragma("unroll") for (int e = 0; e < 2; ++e) {                              \
      a[buf][e] = *(const bf16x8*)(pa + (2 * (q) + e) * 32);                     \
      b1[buf][e] = *(const bf16x8*)(pb1 + e * (2 * UB_S * 16) + (q) * 16);       \
      if (full) b0[buf][e] = *(const bf16x8*)(pb0 + e * (2 * UB_S * 16) + (q) * 16); \
    }                                                                            \
  }
#define CONV_MM(buf, full)                                                                        \
  {                                                                                               \
    _Pragma("unroll") for (int e = 0; e < 2; ++e) {                                               \
      acc1 = __builtin_amdgcn_mfma_f32_32x32x16_bf16(a[buf][e], b1[buf][e], acc1, 0, 0, 0);       \
      if (full) acc0 = __builtin_amdgcn_mfma_f32_32x32x16_bf16(a[buf][e], b0[buf][e], acc0, 0, 0, 0); \
    }                                                                                             \
  }
    CONV_LD(0, 0, true);
    CONV_LD(1, 1, true);
    for (int q = 0; q < 30; q += 3) {
      CONV_LD(2, q + 2, true); SB(); CONV_MM(0, true); SB();
      CONV_LD(0, q + 3, true); SB(); CONV_MM(1, true); SB();
      if (q + 4 < 32) { CONV_LD(1, q + 4, true); } else { CONV_LD(1, q + 4, false); }
      SB(); CONV_MM(2, true); SB();
    }
    CONV_LD(2, 32, false); SB(); CONV_MM(0, true); SB();
    CONV_LD(0, 33, false); SB(); CONV_MM(1, true); SB();
    for (int q = 32; q < 62; q += 3) {
      CONV_LD(1, q + 2, false); SB(); CONV_MM(2, false); SB();
      CONV_LD(2, q + 3, false); SB(); CONV_MM(0, false); SB();
      if (q + 4 < 64) { CONV_LD(0, q + 4, false); }
      SB(); CONV_MM(1, false); SB();
    }
    CONV_MM(2, false); SB();
    CONV_MM(0, false); SB();
    STAMP(3, 2);
    char* yb = smem + wave * HC_CH;
#pragma unroll
    for (int q4 = 0; q4 < 4; ++q4) {
      u32x2 o0 = {pack_bf16(acc0[4 * q4], acc0[4 * q4 + 1]), pack_bf16(acc0[4 * q4 + 2], acc0[4 * q4 + 3])};
      u32x2 o1 = {pack_bf16(acc1[4 * q4], acc1[4 * q4 + 1]), pack_bf16(acc1[4 * q4 + 2], acc1[4 * q4 + 3])};
      *(u32x2*)(yb + (32 * i + 8 * q4 + 4 * hh) * 2) = o0;
      *(u32x2*)(yb + (32 * (32 + i) + 8 * q4 + 4 * hh) * 2) = o1;
    }
  }
  __syncthreads();
  {
    u32x2 dd = *(const u32x2*)(ssmD + d0);
    float yv[4][2];
    int cidx = 255 - (tid >> 2);
    int off = ((cidx & 3) * UB_S + (cidx >> 2)) * 16 + (3 - (tid & 3)) * 4;
#pragma unroll
    for (int ch = 0; ch < 4; ++ch) {
      unsigned c = *(const unsigned*)(smem + ch * HC_CH + tid * 4);
      unsigned kvp = *(const unsigned*)(ubase + ch * UB_CH + off);
      float dv = chsel(dd, ch);
      yv[ch][0] = rbf(bflo(c) + rbf(bfhi(kvp) * dv));
      yv[ch][1] = rbf(bfhi(c) + rbf(bflo(kvp) * dv));
    }
    char* yo = ws + OFF_Y + ((size_t)cg * 2048 + s0) * 8;
    u32x4 o = {pack_bf16(yv[0][0], yv[1][0]), pack_bf16(yv[2][0], yv[3][0]), pack_bf16(yv[0][1], yv[1][1]),
               pack_bf16(yv[2][1], yv[3][1])};
    *(u32x4*)yo = o;
  }
  STAMP(3, 3);
}

DEVI void kvt_part(const u16* __restrict__ kin, const u16* __restrict__ vin, const u16* __restrict__ shiftk,
                   char* __restrict__ ws, int id) {
  const int lane = id & 63, wv = id >> 6;
  const int cgp = wv % 9, tt = wv / 9;
  const int tb = lane >> 3, cc = lane & 7;
  const int s0 = tt * 32 + tb * 4, c0 = cgp * 64 + cc * 8;
  u32x4 kr[7], vr[4], wr[4];
#pragma unroll
  for (int i = 0; i < 7; ++i) {
    int s = s0 - 3 + i;
    s = s < 0 ? 0 : s;
    kr[i] = *(const u32x4*)(kin + (size_t)s * 576 + c0);
  }
#pragma unroll
  for (int i = 0; i < 4; ++i) vr[i] = *(const u32x4*)(vin + (size_t)(s0 + i) * 576 + c0);
#pragma unroll
  for (int i = 0; i < 4; ++i) wr[i] = *(const u32x4*)(shiftk + (size_t)c0 * 4 + i * 8);
  if (s0 == 0) {
    u32x4 z = {0u, 0u, 0u, 0u};
    kr[0] = z; kr[1] = z; kr[2] = z;
  }
#pragma unroll
  for (int ch = 0; ch < 8; ++ch) {
    unsigned w01 = wr[ch >> 1][(ch & 1) * 2], w23 = wr[ch >> 1][(ch & 1) * 2 + 1];
    float w[4] = {bflo(w01), bfhi(w01), bflo(w23), bfhi(w23)};
    float kv[4];
#pragma unroll
    for (int i = 0; i < 4; ++i) {
      float acc = 0.f;
#pragma unroll
      for (int j = 3; j >= 0; --j) {
        unsigned kw = kr[i + 3 - j][ch >> 1];
        acc = rbf(acc + rbf(((ch & 1) ? bfhi(kw) : bflo(kw)) * w[j]));
      }
      unsigned vw = vr[i][ch >> 1];
      kv[i] = acc * ((ch & 1) ? bfhi(vw) : bflo(vw));
    }
    u32x2 o = {pack_bf16(kv[3], kv[2]), pack_bf16(kv[1], kv[0])};
    *(u32x2*)(ws + OFF_KVT + ((size_t)(c0 + ch) * 2048 + 2044 - s0) * 2) = o;
  }
}

constexpr int NT_KVT = 2048 * 576 / 32;
__global__ __launch_bounds__(256) void k_pre(const u16* __restrict__ kin, const u16* __restrict__ vin,
                                             const u16* __restrict__ shiftk, char* __restrict__ ws) {
  kvt_part(kin, vin, shiftk, ws, blockIdx.x * 256 + threadIdx.x);
}

DEVI void layernorm8(const float (&x)[8], float (&o)[8], float2* red, int wave, int t8) {
  float m = 0.f;
#pragma unroll
  for (int j = 0; j < 8; ++j) m += x[j];
  m *= 0.125f;
  float M2 = 0.f;
#pragma unroll
  for (int j = 0; j < 8; ++j) {
    float d = x[j] - m;
    M2 += d * d;
  }
  float n2 = 4.f;
#pragma unroll
  for (int sh = 8; sh <= 32; sh <<= 1) {
    float mo = __shfl_xor(m, sh), M2o = __shfl_xor(M2, sh);
    float d = mo - m;
    M2 = M2 + M2o + d * d * n2;
    m = 0.5f * (m + mo);
    n2 *= 2.f;
  }
  if ((threadIdx.x & 63) < 8) red[wave * 8 + t8] = make_float2(m, M2);
  __syncthreads();
  float2 pr[9];
  float mean = 0.f;
#pragma unroll
  for (int w = 0; w < 9; ++w) {
    pr[w] = red[w * 8 + t8];
    mean += pr[w].x;
  }
  mean *= (1.0f / 9.0f);
  float tq = 0.f;
#pragma unroll
  for (int w = 0; w < 9; ++w) {
    float d = pr[w].x - mean;
    tq += pr[w].y + 64.f * d * d;
  }
  float var = rbf(tq * (1.0f / 576.0f));
  float mb = rbf(mean);
  float sd = rbf(__builtin_amdgcn_sqrtf(rbf(var + 1e-5f)));
  float rs = __builtin_amdgcn_rcpf(sd);
#pragma unroll
  for (int j = 0; j < 8; ++j) o[j] = rbf(rbf(x[j] - mb) * rs);
}

template <int MODE>
__global__ __launch_bounds__(576) void k_row(const u16* __restrict__ q, char* __restrict__ ws, u16* __restrict__ out) {
  __shared__ float2 red[2][72];
  const int tid = threadIdx.x, lane = tid & 63, wave = tid >> 6;
  const int T = blockIdx.x >> 1, half = blockIdx.x & 1;
  const int kp = lane >> 5, G = (lane >> 3) & 3, t8 = lane & 7;
  const int tok = half * 8 + t8, token = T * 16 + tok;
  const int kt = 2 * wave + kp;
  const int chunk = G * 16 + tok;
  const size_t fragoff = ((size_t)(T * 18 + kt) * 64 + chunk) * 16;

  float x[8];
  if (MODE == 0) {
    u32x4 c = *(const u32x4*)(q + (size_t)token * 576 + kt * 32 + G * 8);
    x[0] = bflo(c.x); x[1] = bfhi(c.x); x[2] = bflo(c.y); x[3] = bfhi(c.y);
    x[4] = bflo(c.z); x[5] = bfhi(c.z); x[6] = bflo(c.w); x[7] = bfhi(c.w);
  } else {
    f32x4 pa = {0.f, 0.f, 0.f, 0.f}, pb = {0.f, 0.f, 0.f, 0.f};
#pragma unroll
    for (int sp = 0; sp < 4; ++sp) {
      const char* pp = ws + OFF_P + ((size_t)((sp * 128 + T) * 36 + 2 * kt) * 64 + chunk) * 16;
      pa += *(const f32x4*)pp;
      pb += *(const f32x4*)(pp + 1024);
    }
    u32x4 c = *(const u32x4*)(ws + (MODE == 1 ? OFF_X1 : OFF_X3) + fragoff);
    float xp[8] = {bflo(c.x), bfhi(c.x), bflo(c.y), bfhi(c.y), bflo(c.z), bfhi(c.z), bflo(c.w), bfhi(c.w)};
#pragma unroll
    for (int j = 0; j < 4; ++j) {
      x[j] = rbf(xp[j] + rbf(pa[j]));
      x[4 + j] = rbf(xp[4 + j] + rbf(pb[j]));
    }
  }
  if (MODE == 2) {
    u32x4 o = {pack_bf16(x[0], x[1]), pack_bf16(x[2], x[3]), pack_bf16(x[4], x[5]), pack_bf16(x[6], x[7])};
    *(u32x4*)(out + (size_t)token * 576 + kt * 32 + G * 8) = o;
    return;
  }
  u32x2 ya = *(const u32x2*)(ws + OFF_Y + ((size_t)(8 * kt + 2 * G) * 2048 + token) * 8);
  u32x2 yb = *(const u32x2*)(ws + OFF_Y + ((size_t)(8 * kt + 2 * G + 1) * 2048 + token) * 8);
  float yv[8] = {bflo(ya.x), bfhi(ya.x), bflo(ya.y), bfhi(ya.y), bflo(yb.x), bfhi(yb.x), bflo(yb.y), bfhi(yb.y)};
  float a[8];
  layernorm8(x, a, red[0], wave, t8);
  float xn[8];
#pragma unroll
  for (int j = 0; j < 8; ++j) xn[j] = rbf(x[j] + rbf(a[j] * yv[j]));
  {
    u32x4 o = {pack_bf16(xn[0], xn[1]), pack_bf16(xn[2], xn[3]), pack_bf16(xn[4], xn[5]), pack_bf16(xn[6], xn[7])};
    *(u32x4*)(ws + (MODE == 0 ? OFF_X1 : OFF_X3) + fragoff) = o;
  }
  float b[8];
  layernorm8(xn, b, red[1], wave, t8);
  {
    u32x4 o = {pack_bf16(b[0], b[1]), pack_bf16(b[2], b[3]), pack_bf16(b[4], b[5]), pack_bf16(b[6], b[7])};
    size_t off = ((size_t)(((T >> 4) * 18 + kt) * 16 + (T & 15)) * 64 + chunk) * 16;
    *(u32x4*)(ws + OFF_XN + off) = o;
  }
}

constexpr int STAGE_B = 34 * 1024;
constexpr int NSLOT = 4;
constexpr int GEMM_LDS = NSLOT * STAGE_B;

struct G1State {
  f32x4 acc[9][4];
  bf16x8 xf[2][4];
  bf16x8 wf[9];
};

template <int PIECE>
DEVI void g1_piece(const char* px, const char* pw, unsigned l, int tid, int wave) {
  if (PIECE == 0) aglds16(px + tid * 16, l + wave * 1024);
  if (PIECE == 1) aglds16(px + 8192 + tid * 16, l + 8192 + wave * 1024);
  if (PIECE == 2) aglds16(pw + tid * 16, l + 16384 + wave * 1024);
  if (PIECE == 3) aglds16(pw + 8192 + tid * 16, l + 24576 + wave * 1024);
  if (PIECE == 4) aglds4(pw + 16384 + tid * 4, l + 32768 + wave * 256);
}
#define G1_MM(f) _Pragma("unroll") for (int t = 0; t < 4; ++t) \
    st.acc[f][t] = __builtin_amdgcn_mfma_f32_16x16x32_bf16(st.wf[f], st.xf[S & 1][t], st.acc[f][t], 0, 0, 0)
template <int S>
DEVI void g1_step(G1State& st, const char* px, const char* pw, char* smem, unsigned lbase, int tid, int wave,
                  const char* lx, const char* lw) {
  constexpr int NS = 18;
  if (S + 1 < NS) {
    wait_vm<(S + 2 < NS) ? 5 : 0>();
    bar();
  }
  SB();
  const char* cw = lw + (S % NSLOT) * STAGE_B;
  const char* gx = px + (S + 3) * 16384;
  const char* gw = pw + (S + 3) * 18432;
  const unsigned l = lbase + ((S + 3) % NSLOT) * STAGE_B;
  constexpr bool ISS = (S + 3 < NS);
  G1_MM(0); st.wf[5] = *(const bf16x8*)(cw + 5 * 1024); if (ISS) g1_piece<0>(gx, gw, l, tid, wave); SB();
  G1_MM(1); st.wf[6] = *(const bf16x8*)(cw + 6 * 1024); if (ISS) g1_piece<1>(gx, gw, l, tid, wave); SB();
  G1_MM(2); st.wf[7] = *(const bf16x8*)(cw + 7 * 1024); if (ISS) g1_piece<2>(gx, gw, l, tid, wave); SB();
  G1_MM(3); st.wf[8] = *(const bf16x8*)(cw + 8 * 1024); if (ISS) g1_piece<3>(gx, gw, l, tid, wave); SB();
  G1_MM(4); if (ISS) g1_piece<4>(gx, gw, l, tid, wave); SB();
  const char* nx = lx + ((S + 1) % NSLOT) * STAGE_B;
  const char* nw = lw + ((S + 1) % NSLOT) * STAGE_B;
  constexpr bool NXT = (S + 1 < NS);
  G1_MM(5);
  if (NXT) {
    st.xf[(S + 1) & 1][0] = *(const bf16x8*)(nx);
    st.xf[(S + 1) & 1][1] = *(const bf16x8*)(nx + 1024);
    st.wf[0] = *(const bf16x8*)(nw);
  }
  SB();
  G1_MM(6);
  if (NXT) {
    st.xf[(S + 1) & 1][2] = *(const bf16x8*)(nx + 2048);
    st.xf[(S + 1) & 1][3] = *(const bf16x8*)(nx + 3072);
    st.wf[1] = *(const bf16x8*)(nw + 1024);
  }
  SB();
  G1_MM(7);
  if (NXT) {
    st.wf[2] = *(const bf16x8*)(nw + 2048);
    st.wf[3] = *(const bf16x8*)(nw + 3072);
    st.wf[4] = *(const bf16x8*)(nw + 4096);
  }
  SB();
  G1_MM(8);
  SB();
  if constexpr (S + 1 < NS) g1_step<S + 1>(st, px, pw, smem, lbase, tid, wave, lx, lw);
}

DEVI float silu_f(float g) { return g * __builtin_amdgcn_rcpf(1.0f + __builtin_amdgcn_exp2f(g * -1.44269504088896f)); }

typedef __attribute__((ext_vector_type(3))) unsigned int u32x3;
template <int BM4>
DEVI void g1_epilogue(const G1State& st, char* hb) {
#pragma unroll
  for (int t = 0; t < 4; ++t) {
    unsigned d[9];
#pragma unroll
    for (int f = 0; f < 9; ++f) {
      f32x2 g = {st.acc[f][t][0], st.acc[f][t][1]};
      f32x2 u = {st.acc[f][t][2], st.acc[f][t][3]};
      f32x2 e = g * -1.44269504088896f;
      e[0] = __builtin_amdgcn_exp2f(e[0]);
      e[1] = __builtin_amdgcn_exp2f(e[1]);
      e = e + 1.0f;
      e[0] = __builtin_amdgcn_rcpf(e[0]);
      e[1] = __builtin_amdgcn_rcpf(e[1]);
      f32x2 h = (g * e) * u;
      d[f] = pack_bf16(h[0], h[1]);
    }
    char* p = hb + (size_t)t * 144 * 1024;
    if (BM4 == 0) {
      *(u32x4*)(p) = u32x4{d[0], d[1], d[2], d[3]};
      *(u32x4*)(p + 1024) = u32x4{d[4], d[5], d[6], d[7]};
      *(unsigned*)(p + 2048) = d[8];
    } else if (BM4 == 1) {
      *(u32x3*)(p + 4) = u32x3{d[0], d[1], d[2]};
      *(u32x4*)(p + 1024) = u32x4{d[3], d[4], d[5], d[6]};
      *(u32x2*)(p + 2048) = u32x2{d[7], d[8]};
    } else if (BM4 == 2) {
      *(u32x2*)(p + 8) = u32x2{d[0], d[1]};
      *(u32x4*)(p + 1024) = u32x4{d[2], d[3], d[4], d[5]};
      *(u32x3*)(p + 2048) = u32x3{d[6], d[7], d[8]};
    } else {
      *(unsigned*)(p + 12) = d[0];
      *(u32x4*)(p + 1024) = u32x4{d[1], d[2], d[3], d[4]};
      *(u32x4*)(p + 2048) = u32x4{d[5], d[6], d[7], d[8]};
    }
  }
}

__global__ __launch_bounds__(512, 2) void k_gemm1(char* __restrict__ ws) {
  extern __shared__ __attribute__((aligned(16))) char smem[];
  const int tid = threadIdx.x, lane = tid & 63;
  const int wave = __builtin_amdgcn_readfirstlane(tid >> 6);
  const int bid = blockIdx.x, xcd = bid & 7, idx = bid >> 3;
  const int mb = (xcd & 1) * 4 + (idx & 3), nb = (xcd >> 1) * 8 + (idx >> 2);
  const int wm = wave & 3, wn = wave >> 2;
  const char* px = ws + OFF_XN + (size_t)mb * 18 * 16384;
  const char* pw = ws + OFF_W13 + (size_t)nb * 18 * 18432;
  const char* lx = smem + wm * 4096 + lane * 16;
  const char* lw = smem + 16384 + wn * 9216 + lane * 16;

  STAMP(1, 0);
  G1State st;
#pragma unroll
  for (int f = 0; f < 9; ++f)
#pragma unroll
    for (int t = 0; t < 4; ++t) st.acc[f][t] = f32x4{0.f, 0.f, 0.f, 0.f};

  const unsigned lbase = lds_addr(smem);
#pragma unroll
  for (int s3 = 0; s3 < 3; ++s3) {
    g1_piece<0>(px + s3 * 16384, pw + s3 * 18432, lbase + s3 * STAGE_B, tid, wave);
    g1_piece<1>(px + s3 * 16384, pw + s3 * 18432, lbase + s3 * STAGE_B, tid, wave);
    g1_piece<2>(px + s3 * 16384, pw + s3 * 18432, lbase + s3 * STAGE_B, tid, wave);
    g1_piece<3>(px + s3 * 16384, pw + s3 * 18432, lbase + s3 * STAGE_B, tid, wave);
    g1_piece<4>(px + s3 * 16384, pw + s3 * 18432, lbase + s3 * STAGE_B, tid, wave);
  }
  wait_vm<10>();
  bar();
  STAMP(1, 1);
#pragma unroll
  for (int t = 0; t < 4; ++t) st.xf[0][t] = *(const bf16x8*)(lx + t * 1024);
#pragma unroll
  for (int f = 0; f < 5; ++f) st.wf[f] = *(const bf16x8*)(lw + f * 1024);
  g1_step<0>(st, px, pw, smem, lbase, tid, wave, lx, lw);

  STAMP(1, 2);
  char* hout = ws + OFF_H;
  {
    const int base = nb * 18 + wn * 9;
    char* hb = hout + ((size_t)((mb * 16 + wm * 4) * 144 + (base >> 2)) * 64 + lane) * 16;
    switch (base & 3) {
      case 0: g1_epilogue<0>(st, hb); break;
      case 1: g1_epilogue<1>(st, hb); break;
      case 2: g1_epilogue<2>(st, hb); break;
      default: g1_epilogue<3>(st, hb); break;
    }
  }
  STAMP(1, 3);
}

constexpr int STAGE2_B = 18 * 1024;
struct G2State {
  f32x4 acc[9][2];
  bf16x8 hf[4][2];
  bf16x8 wf[2][9];
};

DEVI void gload16(bf16x8& d, const char* p) {
  asm volatile("global_load_dwordx4 %0, %1, off" : "=v"(d) : "v"(p) : "memory");
}

template <int R, int PIECE>
DEVI void g2_piece(G2State& st, const char* ph, const char* pw, unsigned lbase, int tid, int wave) {
  const unsigned l = lbase + R * STAGE2_B;
  if (PIECE == 0) aglds16(pw + tid * 16, l + wave * 1024);
  if (PIECE == 1) aglds16(pw + 8192 + tid * 16, l + 8192 + wave * 1024);
  if (PIECE == 2) aglds4(pw + 16384 + tid * 4, l + 16384 + wave * 256);
  if (PIECE == 3) gload16(st.hf[R][0], ph);
  if (PIECE == 4) gload16(st.hf[R][1], ph + 144 * 1024);
}
#define G2_MM(f) _Pragma("unroll") for (int t = 0; t < 2; ++t) \
    st.acc[f][t] = __builtin_amdgcn_mfma_f32_16x16x32_bf16(st.wf[S & 1][f], st.hf[S & 3][t], st.acc[f][t], 0, 0, 0)
template <int S>
DEVI void g2_step(G2State& st, const char* ph, const char* pw, unsigned lbase, int tid, int wave, const char* lw) {
  constexpr int NS = 18;
  if (S + 1 < NS) {
    asm volatile("s_waitcnt vmcnt(%2)" : "+v"(st.hf[(S + 1) & 3][0]), "+v"(st.hf[(S + 1) & 3][1]) : "n"((S + 2 < NS) ? 5 : 0) : "memory");
    bar();
  }
  SB();
  constexpr bool NXT = (S + 1 < NS), ISS = (S + 3 < NS);
  constexpr int R = (S + 3) & 3;
  const char* cw = lw + ((S + 1) % NSLOT) * STAGE2_B;
  const char* gh = ph + (S + 3) * 2048;
  const char* gw = pw + (S + 3) * 18432;
  G2_MM(0); if (NXT) { st.wf[(S + 1) & 1][0] = *(const bf16x8*)(cw); st.wf[(S + 1) & 1][1] = *(const bf16x8*)(cw + 1024); } if (ISS) g2_piece<R, 0>(st, gh, gw, lbase, tid, wave); SB();
  G2_MM(1); if (NXT) { st.wf[(S + 1) & 1][2] = *(const bf16x8*)(cw + 2048); st.wf[(S + 1) & 1][3] = *(const bf16x8*)(cw + 3072); } if (ISS) g2_piece<R, 1>(st, gh, gw, lbase, tid, wave); SB();
  G2_MM(2); if (NXT) { st.wf[(S + 1) & 1][4] = *(const bf16x8*)(cw + 4096); st.wf[(S + 1) & 1][5] = *(const bf16x8*)(cw + 5120); } if (ISS) g2_piece<R, 2>(st, gh, gw, lbase, tid, wave); SB();
  G2_MM(3); if (NXT) { st.wf[(S + 1) & 1][6] = *(const bf16x8*)(cw + 6144); st.wf[(S + 1) & 1][7] = *(const bf16x8*)(cw + 7168); } if (ISS) g2_piece<R, 3>(st, gh, gw, lbase, tid, wave); SB();
  G2_MM(4); if (NXT) { st.wf[(S + 1) & 1][8] = *(const bf16x8*)(cw + 8192); } if (ISS) g2_piece<R, 4>(st, gh, gw, lbase, tid, wave); SB();
  G2_MM(5); G2_MM(6); G2_MM(7); G2_MM(8);
  SB();
  if constexpr (S + 1 < NS) g2_step<S + 1>(st, ph, pw, lbase, tid, wave, lw);
}

__global__ __launch_bounds__(512, 2) void k_gemm2(char* __restrict__ ws) {
  extern __shared__ __attribute__((aligned(16))) char smem[];
  const int tid = threadIdx.x, lane = tid & 63;
  const int wave = __builtin_amdgcn_readfirstlane(tid >> 6);
  const int bid = blockIdx.x, xcd = bid & 7, idx = bid >> 3;
  const int sp = xcd >> 1, mt = (xcd & 1) * 8 + (idx & 7), nb2 = idx >> 3;
  const int kg = wave >> 2, wt = wave & 3;
  const char* ph = ws + OFF_H + ((size_t)((mt * 8 + 2 * wt) * 144 + sp * 36 + kg) * 64 + lane) * 16;
  const char* pw = ws + OFF_W2 + (size_t)(nb2 * 144 + sp * 36) * 9216;
  const char* lw = smem + kg * 9216 + lane * 16;

  STAMP(2, 0);
  G2State st;
#pragma unroll
  for (int f = 0; f < 9; ++f)
#pragma unroll
    for (int t = 0; t < 2; ++t) st.acc[f][t] = f32x4{0.f, 0.f, 0.f, 0.f};

  const unsigned lbase = lds_addr(smem);
  g2_piece<0, 0>(st, ph, pw, lbase, tid, wave); g2_piece<0, 1>(st, ph, pw, lbase, tid, wave); g2_piece<0, 2>(st, ph, pw, lbase, tid, wave);
  g2_piece<0, 3>(st, ph, pw, lbase, tid, wave); g2_piece<0, 4>(st, ph, pw, lbase, tid, wave);
  g2_piece<1, 0>(st, ph + 2048, pw + 18432, lbase, tid, wave); g2_piece<1, 1>(st, ph + 2048, pw + 18432, lbase, tid, wave); g2_piece<1, 2>(st, ph + 2048, pw + 18432, lbase, tid, wave);
  g2_piece<1, 3>(st, ph + 2048, pw + 18432, lbase, tid, wave); g2_piece<1, 4>(st, ph + 2048, pw + 18432, lbase, tid, wave);
  g2_piece<2, 0>(st, ph + 4096, pw + 2 * 18432, lbase, tid, wave); g2_piece<2, 1>(st, ph + 4096, pw + 2 * 18432, lbase, tid, wave); g2_piece<2, 2>(st, ph + 4096, pw + 2 * 18432, lbase, tid, wave);
  g2_piece<2, 3>(st, ph + 4096, pw + 2 * 18432, lbase, tid, wave); g2_piece<2, 4>(st, ph + 4096, pw + 2 * 18432, lbase, tid, wave);
  asm volatile("s_waitcnt vmcnt(10)" : "+v"(st.hf[0][0]), "+v"(st.hf[0][1]) :: "memory");
  bar();
  STAMP(2, 1);
#pragma unroll
  for (int f = 0; f < 9; ++f) st.wf[0][f] = *(const bf16x8*)(lw + f * 1024);
  g2_step<0>(st, ph, pw, lbase, tid, wave, lw);
  STAMP(2, 2);
  __syncthreads();
  char* xb = smem + (wt * 18) * 1024 + lane * 16;
  if (kg == 1) {
#pragma unroll
    for (int f = 0; f < 5; ++f)
#pragma unroll
      for (int t = 0; t < 2; ++t) *(f32x4*)(xb + (f * 2 + t) * 1024) = st.acc[f][t];
  } else {
#pragma unroll
    for (int f = 5; f < 9; ++f)
#pragma unroll
      for (int t = 0; t < 2; ++t) *(f32x4*)(xb + (f * 2 + t) * 1024) = st.acc[f][t];
  }
  __syncthreads();
  {
    char* pout = ws + OFF_P;
#pragma unroll
    for (int f = 0; f < 9; ++f) {
      if ((f < 5) == (kg == 0)) {
#pragma unroll
        for (int t = 0; t < 2; ++t) {
          f32x4 o = st.acc[f][t] + *(const f32x4*)(xb + (f * 2 + t) * 1024);
          const int T = mt * 8 + wt * 2 + t;
          *(f32x4*)(pout + ((size_t)((sp * 128 + T) * 36 + nb2 * 9 + f) * 64 + lane) * 16) = o;
        }
      }
    }
  }
  STAMP(2, 3);
}

extern "C" void kernel_launch(void* const* d_in, const int* in_sizes, int n_in, void* d_out, int out_size, void* d_ws,
                              size_t ws_size, hipStream_t stream) {
  const u16* q = (const u16*)d_in[0];
  const u16* kin = (const u16*)d_in[1];
  const u16* vin = (const u16*)d_in[2];
  const u16* shiftk = (const u16*)d_in[3];
  const u16* ssmk = (const u16*)d_in[4];
  const u16* ssmD = (const u16*)d_in[5];
  const u16* w1 = (const u16*)d_in[7];
  const u16* w3 = (const u16*)d_in[8];
  const u16* w2 = (const u16*)d_in[9];
  char* ws = (char*)d_ws;
  u16* out = (u16*)d_out;

  hipFuncSetAttribute((const void*)k_conv, hipFuncAttributeMaxDynamicSharedMemorySize, PREP_LDS);
  hipFuncSetAttribute((const void*)k_gemm1, hipFuncAttributeMaxDynamicSharedMemorySize, GEMM_LDS);
  hipFuncSetAttribute((const void*)k_gemm2, hipFuncAttributeMaxDynamicSharedMemorySize, 4 * STAGE2_B);

  k_pre<<<NT_KVT / 256, 256, 0, stream>>>(kin, vin, shiftk, ws);
  k_conv<<<256, 1024, PREP_LDS, stream>>>(ssmk, ssmD, w1, w3, w2, ws);
  k_row<0><<<256, 576, 0, stream>>>(q, ws, out);
  k_gemm1<<<256, 512, GEMM_LDS, stream>>>(ws);
  k_gemm2<<<256, 512, 4 * STAGE2_B, stream>>>(ws);
  k_row<1><<<256, 576, 0, stream>>>(q, ws, out);
  k_gemm1<<<256, 512, GEMM_LDS, stream>>>(ws);
  k_gemm2<<<256, 512, 4 * STAGE2_B, stream>>>(ws);
  k_row<2><<<256, 576, 0, stream>>>(q, ws, out);
}
```

```cpp
#include <hip/hip_runtime.h>
#include <hip/hip_bf16.h>
#include <stdint.h>

typedef __attribute__((ext_vector_type(8))) short bf16x8;
typedef __attribute__((ext_vector_type(4))) float f32x4;
typedef __attribute__((ext_vector_type(16))) float f32x16;
typedef __attribute__((ext_vector_type(4))) unsigned int u32x4;
typedef __attribute__((ext_vector_type(2))) unsigned int u32x2;
typedef __attribute__((ext_vector_type(2))) __bf16 bf16x2_t;
typedef __attribute__((ext_vector_type(2))) float f32x2;
typedef unsigned short u16;

#define AS1 __attribute__((address_space(1)))
#define AS3 __attribute__((address_space(3)))
#define DEVI __device__ __forceinline__

constexpr size_t SZ_W13 = 576ull * 18 * 1024;
constexpr size_t SZ_W2 = 36ull * 144 * 1024;
constexpr size_t SZ_ACT = 2048ull * 576 * 2;
constexpr size_t SZ_H = 128ull * 144 * 1024;
constexpr size_t SZ_P = 4ull * 128 * 36 * 1024;
constexpr size_t OFF_W13 = 0;
constexpr size_t OFF_W2 = OFF_W13 + SZ_W13;
constexpr size_t OFF_Y = OFF_W2 + SZ_W2;
constexpr size_t OFF_X1 = OFF_Y + SZ_ACT;
constexpr size_t OFF_X3 = OFF_X1 + SZ_ACT;
constexpr size_t OFF_XN = OFF_X3 + SZ_ACT;
constexpr size_t OFF_H = OFF_XN + SZ_ACT;
constexpr size_t OFF_P = OFF_H + SZ_H;
constexpr size_t OFF_KVT = OFF_P + SZ_P;

DEVI unsigned pack_bf16(float lo, float hi) {
  f32x2 v = {lo, hi};
  bf16x2_t r = __builtin_convertvector(v, bf16x2_t);
  return __builtin_bit_cast(unsigned, r);
}
DEVI float bflo(unsigned u) { return __uint_as_float(u << 16); }
DEVI float bfhi(unsigned u) { return __uint_as_float(u & 0xffff0000u); }
DEVI float rbf(float f) { return bflo(pack_bf16(f, f)); }

template <int N> DEVI void wait_vm() { asm volatile("s_waitcnt vmcnt(%0)" ::"n"(N) : "memory"); }
DEVI void bar() { __builtin_amdgcn_s_barrier(); }

#define STAMP(kk, id) do {} while (0)

DEVI void glds16(const void* g, void* l) {
  __builtin_amdgcn_global_load_lds((const AS1 void*)g, (AS3 void*)l, 16, 0, 0);
}
DEVI void glds4(const void* g, void* l) {
  __builtin_amdgcn_global_load_lds((const AS1 void*)g, (AS3 void*)l, 4, 0, 0);
}

DEVI void aglds16(const void* g, unsigned lds) {
  unsigned keep;
  asm volatile("s_mov_b32 %0, m0\n\ts_mov_b32 m0, %2\n\ts_nop 0\n\tglobal_load_lds_dwordx4 %1, off\n\ts_mov_b32 m0, %0"
               : "=&s"(keep) : "v"(g), "s"(lds) : "memory");
}
DEVI void aglds4(const void* g, unsigned lds) {
  unsigned keep;
  asm volatile("s_mov_b32 %0, m0\n\ts_mov_b32 m0, %2\n\ts_nop 0\n\tglobal_load_lds_dword %1, off\n\ts_mov_b32 m0, %0"
               : "=&s"(keep) : "v"(g), "s"(lds) : "memory");
}
DEVI unsigned lds_addr(const void* p) { return (unsigned)(size_t)(const AS3 char*)p; }

#define SB() __builtin_amdgcn_sched_barrier(0)
#define WAIT_LGKM0() __builtin_amdgcn_s_waitcnt(0xC07F)
constexpr int HC_STRIDE = 260 * 16;
constexpr int HC_CH = 8 * HC_STRIDE;
constexpr int UB_S = 98;
constexpr int UB_CH = 4 * UB_S * 16;
constexpr int PREP_LDS = 4 * HC_CH + 4 * UB_CH;

DEVI float chsel(u32x2 r, int ch) { return ch == 0 ? bflo(r.x) : ch == 1 ? bfhi(r.x) : ch == 2 ? bflo(r.y) : bfhi(r.y); }

DEVI void transpose8(const u32x4 (&r)[8], u32x4 (&o)[8]) {
#pragma unroll
  for (int c = 0; c < 8; ++c) {
    const unsigned sel = (c & 1) ? 0x07060302u : 0x05040100u;
    unsigned d[4];
#pragma unroll
    for (int i = 0; i < 4; ++i) d[i] = __builtin_amdgcn_perm(r[2 * i + 1][c >> 1], r[2 * i][c >> 1], sel);
    o[c] = u32x4{d[0], d[1], d[2], d[3]};
  }
}

constexpr int RET_FS = 1024 + 16;
constexpr int NU13 = 18 * 9, NU2 = 144;
constexpr int RET_LDS = 64 * RET_FS;
DEVI void retile_unit(const u16* __restrict__ w1, const u16* __restrict__ w3, const u16* __restrict__ w2,
                      char* __restrict__ ws, char* img, int unit, int tid) {
  u32x4 r[8], o[8];
  const int wave = tid >> 6, lane = tid & 63;
  if (unit >= NU13 + NU2) {
    __syncthreads();
  } else if (unit < NU13) {
    const int kt = unit / 9, fgg = unit % 9;
    const int mat = wave & 1, G = (wave >> 1) & 3;
    const int fg = fgg * 64 + lane;
    const u16* src = (mat ? w3 : w1) + (size_t)(kt * 32 + G * 8) * 4608 + fg * 8;
#pragma unroll
    for (int i = 0; i < 8; ++i) r[i] = *(const u32x4*)(src + (size_t)i * 4608);
    transpose8(r, o);
    char* d = img + lane * RET_FS + (G * 16 + mat * 2) * 16;
#pragma unroll
    for (int c = 0; c < 8; ++c) *(u32x4*)(d + (4 * (c >> 1) + (c & 1)) * 16) = o[c];
    __syncthreads();
#pragma unroll
    for (int n = 0; n < 8; ++n) {
      const int fl = wave * 8 + n;
      const int f2 = fgg * 64 + fl;
      const int nb = f2 / 18, ftl = f2 % 18;
      u32x4 v = *(const u32x4*)(img + fl * RET_FS + lane * 16);
      *(u32x4*)(ws + OFF_W13 + ((size_t)((nb * 18 + kt) * 18 + ftl) * 64 + lane) * 16) = v;
    }
  } else {
    const int kt2 = unit - NU13;
    if (tid < 288) {
      const int G = tid / 72, og = tid % 72;
      const u16* src = w2 + (size_t)(kt2 * 32 + 2 * G) * 576 + og * 8;
#pragma unroll
      for (int i = 0; i < 8; ++i) r[i] = *(const u32x4*)(src + (size_t)(8 * (i >> 1) + (i & 1)) * 576);
      transpose8(r, o);
      const int m = og >> 2, g = og & 3;
#pragma unroll
      for (int e = 0; e < 2; ++e) {
        char* d = img + (2 * m + e) * RET_FS + (G * 16 + 4 * g) * 16;
#pragma unroll
        for (int c = 0; c < 4; ++c) *(u32x4*)(d + c * 16) = o[4 * e + c];
      }
    }
    __syncthreads();
    for (int ot = wave; ot < 36; ot += 8) {
      const int nb2 = ot / 9, otl = ot % 9;
      u32x4 v = *(const u32x4*)(img + ot * RET_FS + lane * 16);
      *(u32x4*)(ws + OFF_W2 + ((size_t)((nb2 * 144 + kt2) * 9 + otl) * 64 + lane) * 16) = v;
    }
  }
  __syncthreads();
}

__global__ __launch_bounds__(1024, 4) void k_conv(const u16* __restrict__ ssmk, const u16* __restrict__ ssmD,
                                                 const u16* __restrict__ w1, const u16* __restrict__ w3,
                                                 const u16* __restrict__ w2, char* __restrict__ ws) {
  extern __shared__ __attribute__((aligned(16))) char smem[];
  if (blockIdx.x >= 144) {
    const int hf = threadIdx.x >> 9;
    const int un = (blockIdx.x - 144) * 2 + hf;
    retile_unit(w1, w3, w2, ws, smem + hf * RET_LDS, un < NU13 ? un : NU13 + NU2, threadIdx.x & 511);
    return;
  }
  const int tid = threadIdx.x, lane = tid & 63;
  const int wave = __builtin_amdgcn_readfirstlane(tid >> 6);
  const int cg = blockIdx.x;
  const int d0 = cg * 4;
  const int s0 = tid * 2;
  char* ubase = smem + 4 * HC_CH;

  STAMP(3, 0);
  {
    const int ch = wave & 3, b8 = wave >> 2;
    aglds16(ws + OFF_KVT + (size_t)(d0 + ch) * 4096 + (4 * lane + b8) * 16, lds_addr(ubase) + ch * UB_CH + b8 * (UB_S * 16));
  }
  {
    const int ch = wave & 3, qq = wave >> 2;
    const u16* hsrc = ssmk + (size_t)(d0 + ch) * 2048;
    char* hb = smem + ch * HC_CH;
#pragma unroll
    for (int n2 = 0; n2 < 2; ++n2) {
      int cp = -4 + lane + 64 * (n2 == 0 ? qq : 4);
      bool act = (n2 == 0) || (qq == 0 && cp <= 254);
      if (act) {
        int c0 = cp < 0 ? 0 : cp, c1 = cp + 1 < 0 ? 0 : cp + 1;
        u32x4 lo = *(const u32x4*)(hsrc + 8 * c0);
        u32x4 hi = *(const u32x4*)(hsrc + 8 * c1);
        u32x4 z = {0u, 0u, 0u, 0u};
        if (cp < 0) lo = z;
        if (cp + 1 < 0) hi = z;
        unsigned Dw[8] = {lo.x, lo.y, lo.z, lo.w, hi.x, hi.y, hi.z, hi.w};
        unsigned Ow[7];
#pragma unroll
        for (int t = 0; t < 7; ++t) Ow[t] = (Dw[t] >> 16) | (Dw[t + 1] << 16);
        char* dst = hb + (cp + 4) * 16;
#pragma unroll
        for (int p = 0; p < 8; ++p) {
          int pp = p + 1;
          u32x4 o;
          if (pp & 1) {
            int e = (pp - 1) >> 1;
            o = u32x4{Ow[e], Ow[e + 1], Ow[e + 2], Ow[e + 3]};
          } else {
            int e = pp >> 1;
            o = u32x4{Dw[e], Dw[e + 1], Dw[e + 2], Dw[e + 3]};
          }
          *(u32x4*)(dst + p * HC_STRIDE) = o;
        }
      }
    }
  }
  if (tid < 512) {
    int ch = tid >> 7, rem = tid & 127;
    u32x4 z = {0u, 0u, 0u, 0u};
    *(u32x4*)(ubase + ch * UB_CH + (((rem >> 5) * UB_S) + 64 + (rem & 31)) * 16) = z;
  }
  wait_vm<0>();
  __syncthreads();
  STAMP(3, 1);

  if (wave < 4) {
    f32x16 acc0 = {}, acc1 = {};
    const int i = lane & 31, hh = lane >> 5;
    const char* pa = smem + wave * HC_CH + (lane & 7) * HC_STRIDE + (hh + (i >> 3)) * 16;
    const char* pb1 = ubase + wave * UB_CH + hh * (UB_S * 16) + (31 - i) * 16;
    const char* pb0 = pb1 + 32 * 16;
    bf16x8 a[3][2], b1[3][2], b0[3][2];
#define CONV_LD(buf, q, full)                                                    \
  {                                                                              \
    _Pragma("unroll") for (int e = 0; e < 2; ++e) {                              \
      a[buf][e] = *(const bf16x8*)(pa + (2 * (q) + e) * 32);                     \
      b1[buf][e] = *(const bf16x8*)(pb1 + e * (2 * UB_S * 16) + (q) * 16);       \
      if (full) b0[buf][e] = *(const bf16x8*)(pb0 + e * (2 * UB_S * 16) + (q) * 16); \
    }                                                                            \
  }
#define CONV_MM(buf, full)                                                                        \
  {                                                                                               \
    _Pragma("unroll") for (int e = 0; e < 2; ++e) {                                               \
      acc1 = __builtin_amdgcn_mfma_f32_32x32x16_bf16(a[buf][e], b1[buf][e], acc1, 0, 0, 0);       \
      if (full) acc0 = __builtin_amdgcn_mfma_f32_32x32x16_bf16(a[buf][e], b0[buf][e], acc0, 0, 0, 0); \
    }                                                                                             \
  }
    CONV_LD(0, 0, true);
    CONV_LD(1, 1, true);
    for (int q = 0; q < 30; q += 3) {
      CONV_LD(2, q + 2, true); SB(); CONV_MM(0, true); SB();
      CONV_LD(0, q + 3, true); SB(); CONV_MM(1, true); SB();
      if (q + 4 < 32) { CONV_LD(1, q + 4, true); } else { CONV_LD(1, q + 4, false); }
      SB(); CONV_MM(2, true); SB();
    }
    CONV_LD(2, 32, false); SB(); CONV_MM(0, true); SB();
    CONV_LD(0, 33, false); SB(); CONV_MM(1, true); SB();
    for (int q = 32; q < 62; q += 3) {
      CONV_LD(1, q + 2, false); SB(); CONV_MM(2, false); SB();
      CONV_LD(2, q + 3, false); SB(); CONV_MM(0, false); SB();
      if (q + 4 < 64) { CONV_LD(0, q + 4, false); }
      SB(); CONV_MM(1, false); SB();
    }
    CONV_MM(2, false); SB();
    CONV_MM(0, false); SB();
    STAMP(3, 2);
    char* yb = smem + wave * HC_CH;
#pragma unroll
    for (int q4 = 0; q4 < 4; ++q4) {
      u32x2 o0 = {pack_bf16(acc0[4 * q4], acc0[4 * q4 + 1]), pack_bf16(acc0[4 * q4 + 2], acc0[4 * q4 + 3])};
      u32x2 o1 = {pack_bf16(acc1[4 * q4], acc1[4 * q4 + 1]), pack_bf16(acc1[4 * q4 + 2], acc1[4 * q4 + 3])};
      *(u32x2*)(yb + (32 * i + 8 * q4 + 4 * hh) * 2) = o0;
      *(u32x2*)(yb + (32 * (32 + i) + 8 * q4 + 4 * hh) * 2) = o1;
    }
  }
  __syncthreads();
  {
    u32x2 dd = *(const u32x2*)(ssmD + d0);
    float yv[4][2];
    int cidx = 255 - (tid >> 2);
    int off = ((cidx & 3) * UB_S + (cidx >> 2)) * 16 + (3 - (tid & 3)) * 4;
#pragma unroll
    for (int ch = 0; ch < 4; ++ch) {
      unsigned c = *(const unsigned*)(smem + ch * HC_CH + tid * 4);
      unsigned kvp = *(const unsigned*)(ubase + ch * UB_CH + off);
      float dv = chsel(dd, ch);
      yv[ch][0] = rbf(bflo(c) + rbf(bfhi(kvp) * dv));
      yv[ch][1] = rbf(bfhi(c) + rbf(bflo(kvp) * dv));
    }
    char* yo = ws + OFF_Y + ((size_t)cg * 2048 + s0) * 8;
    u32x4 o = {pack_bf16(yv[0][0], yv[1][0]), pack_bf16(yv[2][0], yv[3][0]), pack_bf16(yv[0][1], yv[1][1]),
               pack_bf16(yv[2][1], yv[3][1])};
    *(u32x4*)yo = o;
  }
  STAMP(3, 3);
}

DEVI void kvt_part(const u16* __restrict__ kin, const u16* __restrict__ vin, const u16* __restrict__ shiftk,
                   char* __restrict__ ws, int id) {
  const int lane = id & 63, wv = id >> 6;
  const int cgp = wv % 9, tt = wv / 9;
  const int tb = lane >> 3, cc = lane & 7;
  const int s0 = tt * 32 + tb * 4, c0 = cgp * 64 + cc * 8;
  u32x4 kr[7], vr[4], wr[4];
#pragma unroll
  for (int i = 0; i < 7; ++i) {
    int s = s0 - 3 + i;
    s = s < 0 ? 0 : s;
    kr[i] = *(const u32x4*)(kin + (size_t)s * 576 + c0);
  }
#pragma unroll
  for (int i = 0; i < 4; ++i) vr[i] = *(const u32x4*)(vin + (size_t)(s0 + i) * 576 + c0);
#pragma unroll
  for (int i = 0; i < 4; ++i) wr[i] = *(const u32x4*)(shiftk + (size_t)c0 * 4 + i * 8);
  if (s0 == 0) {
    u32x4 z = {0u, 0u, 0u, 0u};
    kr[0] = z; kr[1] = z; kr[2] = z;
  }
#pragma unroll
  for (int ch = 0; ch < 8; ++ch) {
    unsigned w01 = wr[ch >> 1][(ch & 1) * 2], w23 = wr[ch >> 1][(ch & 1) * 2 + 1];
    float w[4] = {bflo(w01), bfhi(w01), bflo(w23), bfhi(w23)};
    float kv[4];
#pragma unroll
    for (int i = 0; i < 4; ++i) {
      float acc = 0.f;
#pragma unroll
      for (int j = 3; j >= 0; --j) {
        unsigned kw = kr[i + 3 - j][ch >> 1];
        acc = rbf(acc + rbf(((ch & 1) ? bfhi(kw) : bflo(kw)) * w[j]));
      }
      unsigned vw = vr[i][ch >> 1];
      kv[i] = acc * ((ch & 1) ? bfhi(vw) : bflo(vw));
    }
    u32x2 o = {pack_bf16(kv[3], kv[2]), pack_bf16(kv[1], kv[0])};
    *(u32x2*)(ws + OFF_KVT + ((size_t)(c0 + ch) * 2048 + 2044 - s0) * 2) = o;
  }
}

constexpr int NT_KVT = 2048 * 576 / 32;
constexpr int NB_KVT = NT_KVT / 512;
__global__ __launch_bounds__(512) void k_pre(const u16* __restrict__ kin, const u16* __restrict__ vin,
                                             const u16* __restrict__ shiftk, const u16* __restrict__ w2,
                                             char* __restrict__ ws) {
  __shared__ __attribute__((aligned(16))) char img[36 * RET_FS];
  if (blockIdx.x < NB_KVT)
    kvt_part(kin, vin, shiftk, ws, blockIdx.x * 512 + threadIdx.x);
  else
    retile_unit(nullptr, nullptr, w2, ws, img, NU13 + blockIdx.x - NB_KVT, threadIdx.x);
}

DEVI void layernorm8(const float (&x)[8], float (&o)[8], float2* red, int wave, int t8) {
  float m = 0.f;
#pragma unroll
  for (int j = 0; j < 8; ++j) m += x[j];
  m *= 0.125f;
  float M2 = 0.f;
#pragma unroll
  for (int j = 0; j < 8; ++j) {
    float d = x[j] - m;
    M2 += d * d;
  }
  float n2 = 4.f;
#pragma unroll
  for (int sh = 8; sh <= 32; sh <<= 1) {
    float mo = __shfl_xor(m, sh), M2o = __shfl_xor(M2, sh);
    float d = mo - m;
    M2 = M2 + M2o + d * d * n2;
    m = 0.5f * (m + mo);
    n2 *= 2.f;
  }
  if ((threadIdx.x & 63) < 8) red[wave * 8 + t8] = make_float2(m, M2);
  __syncthreads();
  float2 pr[9];
  float mean = 0.f;
#pragma unroll
  for (int w = 0; w < 9; ++w) {
    pr[w] = red[w * 8 + t8];
    mean += pr[w].x;
  }
  mean *= (1.0f / 9.0f);
  float tq = 0.f;
#pragma unroll
  for (int w = 0; w < 9; ++w) {
    float d = pr[w].x - mean;
    tq += pr[w].y + 64.f * d * d;
  }
  float var = rbf(tq * (1.0f / 576.0f));
  float mb = rbf(mean);
  float sd = rbf(__builtin_amdgcn_sqrtf(rbf(var + 1e-5f)));
  float rs = __builtin_amdgcn_rcpf(sd);
#pragma unroll
  for (int j = 0; j < 8; ++j) o[j] = rbf(rbf(x[j] - mb) * rs);
}

template <int MODE>
__global__ __launch_bounds__(576) void k_row(const u16* __restrict__ q, char* __restrict__ ws, u16* __restrict__ out) {
  __shared__ float2 red[2][72];
  const int tid = threadIdx.x, lane = tid & 63, wave = tid >> 6;
  const int T = blockIdx.x >> 1, half = blockIdx.x & 1;
  const int kp = lane >> 5, G = (lane >> 3) & 3, t8 = lane & 7;
  const int tok = half * 8 + t8, token = T * 16 + tok;
  const int kt = 2 * wave + kp;
  const int chunk = G * 16 + tok;
  const size_t fragoff = ((size_t)(T * 18 + kt) * 64 + chunk) * 16;

  float x[8];
  if (MODE == 0) {
    u32x4 c = *(const u32x4*)(q + (size_t)token * 576 + kt * 32 + G * 8);
    x[0] = bflo(c.x); x[1] = bfhi(c.x); x[2] = bflo(c.y); x[3] = bfhi(c.y);
    x[4] = bflo(c.z); x[5] = bfhi(c.z); x[6] = bflo(c.w); x[7] = bfhi(c.w);
  } else {
    f32x4 pa = {0.f, 0.f, 0.f, 0.f}, pb = {0.f, 0.f, 0.f, 0.f};
#pragma unroll
    for (int sp = 0; sp < 4; ++sp) {
      const char* pp = ws + OFF_P + ((size_t)((sp * 128 + T) * 36 + 2 * kt) * 64 + chunk) * 16;
      pa += *(const f32x4*)pp;
      pb += *(const f32x4*)(pp + 1024);
    }
    u32x4 c = *(const u32x4*)(ws + (MODE == 1 ? OFF_X1 : OFF_X3) + fragoff);
    float xp[8] = {bflo(c.x), bfhi(c.x), bflo(c.y), bfhi(c.y), bflo(c.z), bfhi(c.z), bflo(c.w), bfhi(c.w)};
#pragma unroll
    for (int j = 0; j < 4; ++j) {
      x[j] = rbf(xp[j] + rbf(pa[j]));
      x[4 + j] = rbf(xp[4 + j] + rbf(pb[j]));
    }
  }
  if (MODE == 2) {
    u32x4 o = {pack_bf16(x[0], x[1]), pack_bf16(x[2], x[3]), pack_bf16(x[4], x[5]), pack_bf16(x[6], x[7])};
    *(u32x4*)(out + (size_t)token * 576 + kt * 32 + G * 8) = o;
    return;
  }
  u32x2 ya = *(const u32x2*)(ws + OFF_Y + ((size_t)(8 * kt + 2 * G) * 2048 + token) * 8);
  u32x2 yb = *(const u32x2*)(ws + OFF_Y + ((size_t)(8 * kt + 2 * G + 1) * 2048 + token) * 8);
  float yv[8] = {bflo(ya.x), bfhi(ya.x), bflo(ya.y), bfhi(ya.y), bflo(yb.x), bfhi(yb.x), bflo(yb.y), bfhi(yb.y)};
  float a[8];
  layernorm8(x, a, red[0], wave, t8);
  float xn[8];
#pragma unroll
  for (int j = 0; j < 8; ++j) xn[j] = rbf(x[j] + rbf(a[j] * yv[j]));
  {
    u32x4 o = {pack_bf16(xn[0], xn[1]), pack_bf16(xn[2], xn[3]), pack_bf16(xn[4], xn[5]), pack_bf16(xn[6], xn[7])};
    *(u32x4*)(ws + (MODE == 0 ? OFF_X1 : OFF_X3) + fragoff) = o;
  }
  float b[8];
  layernorm8(xn, b, red[1], wave, t8);
  {
    u32x4 o = {pack_bf16(b[0], b[1]), pack_bf16(b[2], b[3]), pack_bf16(b[4], b[5]), pack_bf16(b[6], b[7])};
    size_t off = ((size_t)(((T >> 4) * 18 + kt) * 16 + (T & 15)) * 64 + chunk) * 16;
    *(u32x4*)(ws + OFF_XN + off) = o;
  }
}

constexpr int STAGE_B = 34 * 1024;
constexpr int NSLOT = 4;
constexpr int GEMM_LDS = NSLOT * STAGE_B;

struct G1State {
  f32x4 acc[9][4];
  bf16x8 xf[2][4];
  bf16x8 wf[9];
};

template <int PIECE>
DEVI void g1_piece(const char* px, const char* pw, unsigned l, int tid, int wave) {
  if (PIECE == 0) aglds16(px + tid * 16, l + wave * 1024);
  if (PIECE == 1) aglds16(px + 8192 + tid * 16, l + 8192 + wave * 1024);
  if (PIECE == 2) aglds16(pw + tid * 16, l + 16384 + wave * 1024);
  if (PIECE == 3) aglds16(pw + 8192 + tid * 16, l + 24576 + wave * 1024);
  if (PIECE == 4) aglds4(pw + 16384 + tid * 4, l + 32768 + wave * 256);
}
#define G1_MM(f) _Pragma("unroll") for (int t = 0; t < 4; ++t) \
    st.acc[f][t] = __builtin_amdgcn_mfma_f32_16x16x32_bf16(st.wf[f], st.xf[S & 1][t], st.acc[f][t], 0, 0, 0)
template <int S>
DEVI void g1_step(G1State& st, const char* px, const char* pw, char* smem, unsigned lbase, int tid, int wave,
                  const char* lx, const char* lw) {
  constexpr int NS = 18;
  if (S + 1 < NS) {
    wait_vm<(S + 2 < NS) ? 5 : 0>();
    bar();
  }
  SB();
  const char* cw = lw + (S % NSLOT) * STAGE_B;
  const char* gx = px + (S + 3) * 16384;
  const char* gw = pw + (S + 3) * 18432;
  const unsigned l = lbase + ((S + 3) % NSLOT) * STAGE_B;
  constexpr bool ISS = (S + 3 < NS);
  G1_MM(0); st.wf[5] = *(const bf16x8*)(cw + 5 * 1024); if (ISS) g1_piece<0>(gx, gw, l, tid, wave); SB();
  G1_MM(1); st.wf[6] = *(const bf16x8*)(cw + 6 * 1024); if (ISS) g1_piece<1>(gx, gw, l, tid, wave); SB();
  G1_MM(2); st.wf[7] = *(const bf16x8*)(cw + 7 * 1024); if (ISS) g1_piece<2>(gx, gw, l, tid, wave); SB();
  G1_MM(3); st.wf[8] = *(const bf16x8*)(cw + 8 * 1024); if (ISS) g1_piece<3>(gx, gw, l, tid, wave); SB();
  G1_MM(4); if (ISS) g1_piece<4>(gx, gw, l, tid, wave); SB();
  const char* nx = lx + ((S + 1) % NSLOT) * STAGE_B;
  const char* nw = lw + ((S + 1) % NSLOT) * STAGE_B;
  constexpr bool NXT = (S + 1 < NS);
  G1_MM(5);
  if (NXT) {
    st.xf[(S + 1) & 1][0] = *(const bf16x8*)(nx);
    st.xf[(S + 1) & 1][1] = *(const bf16x8*)(nx + 1024);
    st.wf[0] = *(const bf16x8*)(nw);
  }
  SB();
  G1_MM(6);
  if (NXT) {
    st.xf[(S + 1) & 1][2] = *(const bf16x8*)(nx + 2048);
    st.xf[(S + 1) & 1][3] = *(const bf16x8*)(nx + 3072);
    st.wf[1] = *(const bf16x8*)(nw + 1024);
  }
  SB();
  G1_MM(7);
  if (NXT) {
    st.wf[2] = *(const bf16x8*)(nw + 2048);
    st.wf[3] = *(const bf16x8*)(nw + 3072);
    st.wf[4] = *(const bf16x8*)(nw + 4096);
  }
  SB();
  G1_MM(8);
  SB();
  if constexpr (S + 1 < NS) g1_step<S + 1>(st, px, pw, smem, lbase, tid, wave, lx, lw);
}

DEVI float silu_f(float g) { return g * __builtin_amdgcn_rcpf(1.0f + __builtin_amdgcn_exp2f(g * -1.44269504088896f)); }

typedef __attribute__((ext_vector_type(3))) unsigned int u32x3;
template <int BM4>
DEVI void g1_epilogue(const G1State& st, char* hb) {
#pragma unroll
  for (int t = 0; t < 4; ++t) {
    unsigned d[9];
#pragma unroll
    for (int f = 0; f < 9; ++f) {
      f32x2 g = {st.acc[f][t][0], st.acc[f][t][1]};
      f32x2 u = {st.acc[f][t][2], st.acc[f][t][3]};
      f32x2 e = g * -1.44269504088896f;
      e[0] = __builtin_amdgcn_exp2f(e[0]);
      e[1] = __builtin_amdgcn_exp2f(e[1]);
      e = e + 1.0f;
      e[0] = __builtin_amdgcn_rcpf(e[0]);
      e[1] = __builtin_amdgcn_rcpf(e[1]);
      f32x2 h = (g * e) * u;
      d[f] = pack_bf16(h[0], h[1]);
    }
    char* p = hb + (size_t)t * 144 * 1024;
    if (BM4 == 0) {
      *(u32x4*)(p) = u32x4{d[0], d[1], d[2], d[3]};
      *(u32x4*)(p + 1024) = u32x4{d[4], d[5], d[6], d[7]};
      *(unsigned*)(p + 2048) = d[8];
    } else if (BM4 == 1) {
      *(u32x3*)(p + 4) = u32x3{d[0], d[1], d[2]};
      *(u32x4*)(p + 1024) = u32x4{d[3], d[4], d[5], d[6]};
      *(u32x2*)(p + 2048) = u32x2{d[7], d[8]};
    } else if (BM4 == 2) {
      *(u32x2*)(p + 8) = u32x2{d[0], d[1]};
      *(u32x4*)(p + 1024) = u32x4{d[2], d[3], d[4], d[5]};
      *(u32x3*)(p + 2048) = u32x3{d[6], d[7], d[8]};
    } else {
      *(unsigned*)(p + 12) = d[0];
      *(u32x4*)(p + 1024) = u32x4{d[1], d[2], d[3], d[4]};
      *(u32x4*)(p + 2048) = u32x4{d[5], d[6], d[7], d[8]};
    }
  }
}

__global__ __launch_bounds__(512, 2) void k_gemm1(char* __restrict__ ws) {
  extern __shared__ __attribute__((aligned(16))) char smem[];
  const int tid = threadIdx.x, lane = tid & 63;
  const int wave = __builtin_amdgcn_readfirstlane(tid >> 6);
  const int bid = blockIdx.x, xcd = bid & 7, idx = bid >> 3;
  const int mb = (xcd & 1) * 4 + (idx & 3), nb = (xcd >> 1) * 8 + (idx >> 2);
  const int wm = wave & 3, wn = wave >> 2;
  const char* px = ws + OFF_XN + (size_t)mb * 18 * 16384;
  const char* pw = ws + OFF_W13 + (size_t)nb * 18 * 18432;
  const char* lx = smem + wm * 4096 + lane * 16;
  const char* lw = smem + 16384 + wn * 9216 + lane * 16;

  STAMP(1, 0);
  G1State st;
#pragma unroll
  for (int f = 0; f < 9; ++f)
#pragma unroll
    for (int t = 0; t < 4; ++t) st.acc[f][t] = f32x4{0.f, 0.f, 0.f, 0.f};

  const unsigned lbase = lds_addr(smem);
#pragma unroll
  for (int s3 = 0; s3 < 3; ++s3) {
    g1_piece<0>(px + s3 * 16384, pw + s3 * 18432, lbase + s3 * STAGE_B, tid, wave);
    g1_piece<1>(px + s3 * 16384, pw + s3 * 18432, lbase + s3 * STAGE_B, tid, wave);
    g1_piece<2>(px + s3 * 16384, pw + s3 * 18432, lbase + s3 * STAGE_B, tid, wave);
    g1_piece<3>(px + s3 * 16384, pw + s3 * 18432, lbase + s3 * STAGE_B, tid, wave);
    g1_piece<4>(px + s3 * 16384, pw + s3 * 18432, lbase + s3 * STAGE_B, tid, wave);
  }
  wait_vm<10>();
  bar();
  STAMP(1, 1);
#pragma unroll
  for (int t = 0; t < 4; ++t) st.xf[0][t] = *(const bf16x8*)(lx + t * 1024);
#pragma unroll
  for (int f = 0; f < 5; ++f) st.wf[f] = *(const bf16x8*)(lw + f * 1024);
  g1_step<0>(st, px, pw, smem, lbase, tid, wave, lx, lw);

  STAMP(1, 2);
  char* hout = ws + OFF_H;
  {
    const int base = nb * 18 + wn * 9;
    char* hb = hout + ((size_t)((mb * 16 + wm * 4) * 144 + (base >> 2)) * 64 + lane) * 16;
    switch (base & 3) {
      case 0: g1_epilogue<0>(st, hb); break;
      case 1: g1_epilogue<1>(st, hb); break;
      case 2: g1_epilogue<2>(st, hb); break;
      default: g1_epilogue<3>(st, hb); break;
    }
  }
  STAMP(1, 3);
}

constexpr int STAGE2_B = 18 * 1024;
struct G2State {
  f32x4 acc[9][2];
  bf16x8 hf[4][2];
  bf16x8 wf[2][9];
};

DEVI void gload16(bf16x8& d, const char* p) {
  asm volatile("global_load_dwordx4 %0, %1, off" : "=v"(d) : "v"(p) : "memory");
}

template <int R, int PIECE>
DEVI void g2_piece(G2State& st, const char* ph, const char* pw, unsigned lbase, int tid, int wave) {
  const unsigned l = lbase + R * STAGE2_B;
  if (PIECE == 0) aglds16(pw + tid * 16, l + wave * 1024);
  if (PIECE == 1) aglds16(pw + 8192 + tid * 16, l + 8192 + wave * 1024);
  if (PIECE == 2) aglds4(pw + 16384 + tid * 4, l + 16384 + wave * 256);
  if (PIECE == 3) gload16(st.hf[R][0], ph);
  if (PIECE == 4) gload16(st.hf[R][1], ph + 144 * 1024);
}
#define G2_MM(f) _Pragma("unroll") for (int t = 0; t < 2; ++t) \
    st.acc[f][t] = __builtin_amdgcn_mfma_f32_16x16x32_bf16(st.wf[S & 1][f], st.hf[S & 3][t], st.acc[f][t], 0, 0, 0)
template <int S>
DEVI void g2_step(G2State& st, const char* ph, const char* pw, unsigned lbase, int tid, int wave, const char* lw) {
  constexpr int NS = 18;
  if (S + 1 < NS) {
    asm volatile("s_waitcnt vmcnt(%2)" : "+v"(st.hf[(S + 1) & 3][0]), "+v"(st.hf[(S + 1) & 3][1]) : "n"((S + 2 < NS) ? 5 : 0) : "memory");
    bar();
  }
  SB();
  constexpr bool NXT = (S + 1 < NS), ISS = (S + 3 < NS);
  constexpr int R = (S + 3) & 3;
  const char* cw = lw + ((S + 1) % NSLOT) * STAGE2_B;
  const char* gh = ph + (S + 3) * 2048;
  const char* gw = pw + (S + 3) * 18432;
  G2_MM(0); if (NXT) { st.wf[(S + 1) & 1][0] = *(const bf16x8*)(cw); st.wf[(S + 1) & 1][1] = *(const bf16x8*)(cw + 1024); } if (ISS) g2_piece<R, 0>(st, gh, gw, lbase, tid, wave); SB();
  G2_MM(1); if (NXT) { st.wf[(S + 1) & 1][2] = *(const bf16x8*)(cw + 2048); st.wf[(S + 1) & 1][3] = *(const bf16x8*)(cw + 3072); } if (ISS) g2_piece<R, 1>(st, gh, gw, lbase, tid, wave); SB();
  G2_MM(2); if (NXT) { st.wf[(S + 1) & 1][4] = *(const bf16x8*)(cw + 4096); st.wf[(S + 1) & 1][5] = *(const bf16x8*)(cw + 5120); } if (ISS) g2_piece<R, 2>(st, gh, gw, lbase, tid, wave); SB();
  G2_MM(3); if (NXT) { st.wf[(S + 1) & 1][6] = *(const bf16x8*)(cw + 6144); st.wf[(S + 1) & 1][7] = *(const bf16x8*)(cw + 7168); } if (ISS) g2_piece<R, 3>(st, gh, gw, lbase, tid, wave); SB();
  G2_MM(4); if (NXT) { st.wf[(S + 1) & 1][8] = *(const bf16x8*)(cw + 8192); } if (ISS) g2_piece<R, 4>(st, gh, gw, lbase, tid, wave); SB();
  G2_MM(5); G2_MM(6); G2_MM(7); G2_MM(8);
  SB();
  if constexpr (S + 1 < NS) g2_step<S + 1>(st, ph, pw, lbase, tid, wave, lw);
}

__global__ __launch_bounds__(512, 2) void k_gemm2(char* __restrict__ ws) {
  extern __shared__ __attribute__((aligned(16))) char smem[];
  const int tid = threadIdx.x, lane = tid & 63;
  const int wave = __builtin_amdgcn_readfirstlane(tid >> 6);
  const int bid = blockIdx.x, xcd = bid & 7, idx = bid >> 3;
  const int sp = xcd >> 1, mt = (xcd & 1) * 8 + (idx & 7), nb2 = idx >> 3;
  const int kg = wave >> 2, wt = wave & 3;
  const char* ph = ws + OFF_H + ((size_t)((mt * 8 + 2 * wt) * 144 + sp * 36 + kg) * 64 + lane) * 16;
  const char* pw = ws + OFF_W2 + (size_t)(nb2 * 144 + sp * 36) * 9216;
  const char* lw = smem + kg * 9216 + lane * 16;

  STAMP(2, 0);
  G2State st;
#pragma unroll
  for (int f = 0; f < 9; ++f)
#pragma unroll
    for (int t = 0; t < 2; ++t) st.acc[f][t] = f32x4{0.f, 0.f, 0.f, 0.f};

  const unsigned lbase = lds_addr(smem);
  g2_piece<0, 0>(st, ph, pw, lbase, tid, wave); g2_piece<0, 1>(st, ph, pw, lbase, tid, wave); g2_piece<0, 2>(st, ph, pw, lbase, tid, wave);
  g2_piece<0, 3>(st, ph, pw, lbase, tid, wave); g2_piece<0, 4>(st, ph, pw, lbase, tid, wave);
  g2_piece<1, 0>(st, ph + 2048, pw + 18432, lbase, tid, wave); g2_piece<1, 1>(st, ph + 2048, pw + 18432, lbase, tid, wave); g2_piece<1, 2>(st, ph + 2048, pw + 18432, lbase, tid, wave);
  g2_piece<1, 3>(st, ph + 2048, pw + 18432, lbase, tid, wave); g2_piece<1, 4>(st, ph + 2048, pw + 18432, lbase, tid, wave);
  g2_piece<2, 0>(st, ph + 4096, pw + 2 * 18432, lbase, tid, wave); g2_piece<2, 1>(st, ph + 4096, pw + 2 * 18432, lbase, tid, wave); g2_piece<2, 2>(st, ph + 4096, pw + 2 * 18432, lbase, tid, wave);
  g2_piece<2, 3>(st, ph + 4096, pw + 2 * 18432, lbase, tid, wave); g2_piece<2, 4>(st, ph + 4096, pw + 2 * 18432, lbase, tid, wave);
  asm volatile("s_waitcnt vmcnt(10)" : "+v"(st.hf[0][0]), "+v"(st.hf[0][1]) :: "memory");
  bar();
  STAMP(2, 1);
#pragma unroll
  for (int f = 0; f < 9; ++f) st.wf[0][f] = *(const bf16x8*)(lw + f * 1024);
  g2_step<0>(st, ph, pw, lbase, tid, wave, lw);
  STAMP(2, 2);
  __syncthreads();
  char* xb = smem + (wt * 18) * 1024 + lane * 16;
  if (kg == 1) {
#pragma unroll
    for (int f = 0; f < 5; ++f)
#pragma unroll
      for (int t = 0; t < 2; ++t) *(f32x4*)(xb + (f * 2 + t) * 1024) = st.acc[f][t];
  } else {
#pragma unroll
    for (int f = 5; f < 9; ++f)
#pragma unroll
      for (int t = 0; t < 2; ++t) *(f32x4*)(xb + (f * 2 + t) * 1024) = st.acc[f][t];
  }
  __syncthreads();
  {
    char* pout = ws + OFF_P;
#pragma unroll
    for (int f = 0; f < 9; ++f) {
      if ((f < 5) == (kg == 0)) {
#pragma unroll
        for (int t = 0; t < 2; ++t) {
          f32x4 o = st.acc[f][t] + *(const f32x4*)(xb + (f * 2 + t) * 1024);
          const int T = mt * 8 + wt * 2 + t;
          *(f32x4*)(pout + ((size_t)((sp * 128 + T) * 36 + nb2 * 9 + f) * 64 + lane) * 16) = o;
        }
      }
    }
  }
  STAMP(2, 3);
}

extern "C" void kernel_launch(void* const* d_in, const int* in_sizes, int n_in, void* d_out, int out_size, void* d_ws,
                              size_t ws_size, hipStream_t stream) {
  const u16* q = (const u16*)d_in[0];
  const u16* kin = (const u16*)d_in[1];
  const u16* vin = (const u16*)d_in[2];
  const u16* shiftk = (const u16*)d_in[3];
  const u16* ssmk = (const u16*)d_in[4];
  const u16* ssmD = (const u16*)d_in[5];
  const u16* w1 = (const u16*)d_in[7];
  const u16* w3 = (const u16*)d_in[8];
  const u16* w2 = (const u16*)d_in[9];
  char* ws = (char*)d_ws;
  u16* out = (u16*)d_out;

  hipFuncSetAttribute((const void*)k_conv, hipFuncAttributeMaxDynamicSharedMemorySize, PREP_LDS);
  hipFuncSetAttribute((const void*)k_gemm1, hipFuncAttributeMaxDynamicSharedMemorySize, GEMM_LDS);
  hipFuncSetAttribute((const void*)k_gemm2, hipFuncAttributeMaxDynamicSharedMemorySize, 4 * STAGE2_B);

  k_pre<<<NB_KVT + NU2, 512, 0, stream>>>(kin, vin, shiftk, w2, ws);
  k_conv<<<256, 1024, PREP_LDS, stream>>>(ssmk, ssmD, w1, w3, w2, ws);
  k_row<0><<<256, 576, 0, stream>>>(q, ws, out);
  k_gemm1<<<256, 512, GEMM_LDS, stream>>>(ws);
  k_gemm2<<<256, 512, 4 * STAGE2_B, stream>>>(ws);
  k_row<1><<<256, 576, 0, stream>>>(q, ws, out);
  k_gemm1<<<256, 512, GEMM_LDS, stream>>>(ws);
  k_gemm2<<<256, 512, 4 * STAGE2_B, stream>>>(ws);
  k_row<2><<<256, 576, 0, stream>>>(q, ws, out);
}
```

```cpp
#include <hip/hip_runtime.h>
#include <hip/hip_bf16.h>
#include <stdint.h>

typedef __attribute__((ext_vector_type(8))) short bf16x8;
typedef __attribute__((ext_vector_type(4))) float f32x4;
typedef __attribute__((ext_vector_type(16))) float f32x16;
typedef __attribute__((ext_vector_type(4))) unsigned int u32x4;
typedef __attribute__((ext_vector_type(2))) unsigned int u32x2;
typedef __attribute__((ext_vector_type(2))) __bf16 bf16x2_t;
typedef __attribute__((ext_vector_type(2))) float f32x2;
typedef unsigned short u16;

#define AS1 __attribute__((address_space(1)))
#define AS3 __attribute__((address_space(3)))
#define DEVI __device__ __forceinline__

constexpr size_t SZ_W13 = 576ull * 18 * 1024;
constexpr size_t SZ_W2 = 36ull * 144 * 1024;
constexpr size_t SZ_ACT = 2048ull * 576 * 2;
constexpr size_t SZ_H = 128ull * 144 * 1024;
constexpr size_t SZ_P = 4ull * 128 * 36 * 1024;
constexpr size_t OFF_W13 = 0;
constexpr size_t OFF_W2 = OFF_W13 + SZ_W13;
constexpr size_t OFF_Y = OFF_W2 + SZ_W2;
constexpr size_t OFF_X1 = OFF_Y + SZ_ACT;
constexpr size_t OFF_X3 = OFF_X1 + SZ_ACT;
constexpr size_t OFF_XN = OFF_X3 + SZ_ACT;
constexpr size_t OFF_H = OFF_XN + SZ_ACT;
constexpr size_t OFF_P = OFF_H + SZ_H;

DEVI unsigned pack_bf16(float lo, float hi) {
  f32x2 v = {lo, hi};
  bf16x2_t r = __builtin_convertvector(v, bf16x2_t);
  return __builtin_bit_cast(unsigned, r);
}
DEVI float bflo(unsigned u) { return __uint_as_float(u << 16); }
DEVI float bfhi(unsigned u) { return __uint_as_float(u & 0xffff0000u); }
DEVI float rbf(float f) { return bflo(pack_bf16(f, f)); }

template <int N> DEVI void wait_vm() { asm volatile("s_waitcnt vmcnt(%0)" ::"n"(N) : "memory"); }
DEVI void bar() { __builtin_amdgcn_s_barrier(); }

#define STAMP(kk, id) do {} while (0)

DEVI void glds16(const void* g, void* l) {
  __builtin_amdgcn_global_load_lds((const AS1 void*)g, (AS3 void*)l, 16, 0, 0);
}
DEVI void glds4(const void* g, void* l) {
  __builtin_amdgcn_global_load_lds((const AS1 void*)g, (AS3 void*)l, 4, 0, 0);
}

DEVI void aglds16(const void* g, unsigned lds) {
  unsigned keep;
  asm volatile("s_mov_b32 %0, m0\n\ts_mov_b32 m0, %2\n\ts_nop 0\n\tglobal_load_lds_dwordx4 %1, off\n\ts_mov_b32 m0, %0"
               : "=&s"(keep) : "v"(g), "s"(lds) : "memory");
}
DEVI void aglds4(const void* g, unsigned lds) {
  unsigned keep;
  asm volatile("s_mov_b32 %0, m0\n\ts_mov_b32 m0, %2\n\ts_nop 0\n\tglobal_load_lds_dword %1, off\n\ts_mov_b32 m0, %0"
               : "=&s"(keep) : "v"(g), "s"(lds) : "memory");
}
DEVI unsigned lds_addr(const void* p) { return (unsigned)(size_t)(const AS3 char*)p; }

#define SB() __builtin_amdgcn_sched_barrier(0)
#define WAIT_LGKM0() __builtin_amdgcn_s_waitcnt(0xC07F)
constexpr int HC_STRIDE = 260 * 16;
constexpr int HC_CH = 8 * HC_STRIDE;
constexpr int UB_S = 98;
constexpr int UB_CH = 4 * UB_S * 16;
constexpr int PREP_LDS = 4 * HC_CH + 4 * UB_CH;

DEVI float chsel(u32x2 r, int ch) { return ch == 0 ? bflo(r.x) : ch == 1 ? bfhi(r.x) : ch == 2 ? bflo(r.y) : bfhi(r.y); }

DEVI void transpose8(const u32x4 (&r)[8], u32x4 (&o)[8]) {
#pragma unroll
  for (int c = 0; c < 8; ++c) {
    const unsigned sel = (c & 1) ? 0x07060302u : 0x05040100u;
    unsigned d[4];
#pragma unroll
    for (int i = 0; i < 4; ++i) d[i] = __builtin_amdgcn_perm(r[2 * i + 1][c >> 1], r[2 * i][c >> 1], sel);
    o[c] = u32x4{d[0], d[1], d[2], d[3]};
  }
}

constexpr int RET_FS = 1024 + 16;
constexpr int NU13 = 18 * 9, NU2 = 144;
constexpr int RET_LDS = 64 * RET_FS;
DEVI void retile_unit(const u16* __restrict__ w1, const u16* __restrict__ w3, const u16* __restrict__ w2,
                      char* __restrict__ ws, char* img, int unit, int tid) {
  u32x4 r[8], o[8];
  const int wave = tid >> 6, lane = tid & 63;
  if (unit >= NU13 + NU2) {
    __syncthreads();
  } else if (unit < NU13) {
    const int kt = unit / 9, fgg = unit % 9;
    const int mat = wave & 1, G = (wave >> 1) & 3;
    const int fg = fgg * 64 + lane;
    const u16* src = (mat ? w3 : w1) + (size_t)(kt * 32 + G * 8) * 4608 + fg * 8;
#pragma unroll
    for (int i = 0; i < 8; ++i) r[i] = *(const u32x4*)(src + (size_t)i * 4608);
    transpose8(r, o);
    char* d = img + lane * RET_FS + (G * 16 + mat * 2) * 16;
#pragma unroll
    for (int c = 0; c < 8; ++c) *(u32x4*)(d + (4 * (c >> 1) + (c & 1)) * 16) = o[c];
    __syncthreads();
#pragma unroll
    for (int n = 0; n < 8; ++n) {
      const int fl = wave * 8 + n;
      const int f2 = fgg * 64 + fl;
      const int nb = f2 / 18, ftl = f2 % 18;
      u32x4 v = *(const u32x4*)(img + fl * RET_FS + lane * 16);
      *(u32x4*)(ws + OFF_W13 + ((size_t)((nb * 18 + kt) * 18 + ftl) * 64 + lane) * 16) = v;
    }
  } else {
    const int kt2 = unit - NU13;
    if (tid < 288) {
      const int G = tid / 72, og = tid % 72;
      const u16* src = w2 + (size_t)(kt2 * 32 + 2 * G) * 576 + og * 8;
#pragma unroll
      for (int i = 0; i < 8; ++i) r[i] = *(const u32x4*)(src + (size_t)(8 * (i >> 1) + (i & 1)) * 576);
      transpose8(r, o);
      const int m = og >> 2, g = og & 3;
#pragma unroll
      for (int e = 0; e < 2; ++e) {
        char* d = img + (2 * m + e) * RET_FS + (G * 16 + 4 * g) * 16;
#pragma unroll
        for (int c = 0; c < 4; ++c) *(u32x4*)(d + c * 16) = o[4 * e + c];
      }
    }
    __syncthreads();
    for (int ot = wave; ot < 36; ot += 8) {
      const int nb2 = ot / 9, otl = ot % 9;
      u32x4 v = *(const u32x4*)(img + ot * RET_FS + lane * 16);
      *(u32x4*)(ws + OFF_W2 + ((size_t)((nb2 * 144 + kt2) * 9 + otl) * 64 + lane) * 16) = v;
    }
  }
  __syncthreads();
}

__global__ __launch_bounds__(1024, 4) void k_conv(const u16* __restrict__ kin, const u16* __restrict__ vin,
                                                 const u16* __restrict__ shiftk,
                                                 const u16* __restrict__ ssmk, const u16* __restrict__ ssmD,
                                                 const u16* __restrict__ w1, const u16* __restrict__ w3,
                                                 const u16* __restrict__ w2, char* __restrict__ ws) {
  extern __shared__ __attribute__((aligned(16))) char smem[];
  if (blockIdx.x >= 144) {
    const int hf = threadIdx.x >> 9;
#pragma unroll 1
    for (int it = 0; it < 2; ++it) {
      const int base = (blockIdx.x - 144) * 2 + it * 224;
      if (base >= NU13 + NU2) break;
      retile_unit(w1, w3, w2, ws, smem + hf * RET_LDS, base + hf, threadIdx.x & 511);
    }
    return;
  }
  const int tid = threadIdx.x, lane = tid & 63;
  const int wave = __builtin_amdgcn_readfirstlane(tid >> 6);
  const int cg = (blockIdx.x & 7) * 18 + (blockIdx.x >> 3);
  const int d0 = cg * 4;
  const int s0 = tid * 2;
  char* ubase = smem + 4 * HC_CH;

  STAMP(3, 0);
  u32x2 kr[2], vr[2];
#pragma unroll
  for (int i = 0; i < 2; ++i) {
    kr[i] = *(const u32x2*)(kin + (size_t)(s0 + i) * 576 + d0);
    vr[i] = *(const u32x2*)(vin + (size_t)(s0 + i) * 576 + d0);
  }
  u32x4 wa = *(const u32x4*)(shiftk + d0 * 4);
  u32x4 wb = *(const u32x4*)(shiftk + d0 * 4 + 8);
  {
    const int ch = wave & 3, qq = wave >> 2;
    const u16* hsrc = ssmk + (size_t)(d0 + ch) * 2048;
    char* hb = smem + ch * HC_CH;
#pragma unroll
    for (int n2 = 0; n2 < 2; ++n2) {
      int cp = -4 + lane + 64 * (n2 == 0 ? qq : 4);
      bool act = (n2 == 0) || (qq == 0 && cp <= 254);
      if (act) {
        int c0 = cp < 0 ? 0 : cp, c1 = cp + 1 < 0 ? 0 : cp + 1;
        u32x4 lo = *(const u32x4*)(hsrc + 8 * c0);
        u32x4 hi = *(const u32x4*)(hsrc + 8 * c1);
        u32x4 z = {0u, 0u, 0u, 0u};
        if (cp < 0) lo = z;
        if (cp + 1 < 0) hi = z;
        unsigned Dw[8] = {lo.x, lo.y, lo.z, lo.w, hi.x, hi.y, hi.z, hi.w};
        unsigned Ow[7];
#pragma unroll
        for (int t = 0; t < 7; ++t) Ow[t] = (Dw[t] >> 16) | (Dw[t + 1] << 16);
        char* dst = hb + (cp + 4) * 16;
#pragma unroll
        for (int p = 0; p < 8; ++p) {
          int pp = p + 1;
          u32x4 o;
          if (pp & 1) {
            int e = (pp - 1) >> 1;
            o = u32x4{Ow[e], Ow[e + 1], Ow[e + 2], Ow[e + 3]};
          } else {
            int e = pp >> 1;
            o = u32x4{Dw[e], Dw[e + 1], Dw[e + 2], Dw[e + 3]};
          }
          *(u32x4*)(dst + p * HC_STRIDE) = o;
        }
      }
    }
  }
  {
    unsigned k01[4] = {(kr[0].x & 0xffffu) | (kr[1].x << 16), (kr[0].x >> 16) | (kr[1].x & 0xffff0000u),
                       (kr[0].y & 0xffffu) | (kr[1].y << 16), (kr[0].y >> 16) | (kr[1].y & 0xffff0000u)};
#pragma unroll
    for (int ch = 0; ch < 4; ++ch) *(unsigned*)(ubase + ch * UB_CH + 8 + tid * 4) = k01[ch];
    if (tid < 8) *(unsigned*)(ubase + (tid >> 1) * UB_CH + (tid & 1) * 4) = 0u;
    __syncthreads();
    unsigned pa2[4], pb2[4];
#pragma unroll
    for (int ch = 0; ch < 4; ++ch) {
      pa2[ch] = *(const unsigned*)(ubase + ch * UB_CH + tid * 4);
      pb2[ch] = *(const unsigned*)(ubase + ch * UB_CH + tid * 4 + 4);
    }
    __syncthreads();
    unsigned wq[8] = {wa.x, wa.y, wa.z, wa.w, wb.x, wb.y, wb.z, wb.w};
    int cidx = 255 - (tid >> 2);
    int off = ((cidx & 3) * UB_S + (cidx >> 2)) * 16 + (3 - (tid & 3)) * 4;
#pragma unroll
    for (int ch = 0; ch < 4; ++ch) {
      float w[4] = {bflo(wq[2 * ch]), bfhi(wq[2 * ch]), bflo(wq[2 * ch + 1]), bfhi(wq[2 * ch + 1])};
      float kk[5] = {bfhi(pa2[ch]), bflo(pb2[ch]), bfhi(pb2[ch]), bflo(k01[ch]), bfhi(k01[ch])};
      float kv[2];
#pragma unroll
      for (int i = 0; i < 2; ++i) {
        float acc = 0.f;
#pragma unroll
        for (int j = 3; j >= 0; --j) acc = rbf(acc + rbf(kk[i + 3 - j] * w[j]));
        kv[i] = acc * chsel(vr[i], ch);
      }
      *(unsigned*)(ubase + ch * UB_CH + off) = pack_bf16(kv[1], kv[0]);
    }
    if (tid < 512) {
      int ch = tid >> 7, rem = tid & 127;
      u32x4 z = {0u, 0u, 0u, 0u};
      *(u32x4*)(ubase + ch * UB_CH + (((rem >> 5) * UB_S) + 64 + (rem & 31)) * 16) = z;
    }
  }
  __syncthreads();
  STAMP(3, 1);

  if (wave < 4) {
    f32x16 acc0 = {}, acc1 = {};
    const int i = lane & 31, hh = lane >> 5;
    const char* pa = smem + wave * HC_CH + (lane & 7) * HC_STRIDE + (hh + (i >> 3)) * 16;
    const char* pb1 = ubase + wave * UB_CH + hh * (UB_S * 16) + (31 - i) * 16;
    const char* pb0 = pb1 + 32 * 16;
    bf16x8 a[3][2], b1[3][2], b0[3][2];
#define CONV_LD(buf, q, full)                                                    \
  {                                                                              \
    _Pragma("unroll") for (int e = 0; e < 2; ++e) {                              \
      a[buf][e] = *(const bf16x8*)(pa + (2 * (q) + e) * 32);                     \
      b1[buf][e] = *(const bf16x8*)(pb1 + e * (2 * UB_S * 16) + (q) * 16);       \
      if (full) b0[buf][e] = *(const bf16x8*)(pb0 + e * (2 * UB_S * 16) + (q) * 16); \
    }                                                                            \
  }
#define CONV_MM(buf, full)                                                                        \
  {                                                                                               \
    _Pragma("unroll") for (int e = 0; e < 2; ++e) {                                               \
      acc1 = __builtin_amdgcn_mfma_f32_32x32x16_bf16(a[buf][e], b1[buf][e], acc1, 0, 0, 0);       \
      if (full) acc0 = __builtin_amdgcn_mfma_f32_32x32x16_bf16(a[buf][e], b0[buf][e], acc0, 0, 0, 0); \
    }                                                                                             \
  }
    CONV_LD(0, 0, true);
    CONV_LD(1, 1, true);
    for (int q = 0; q < 30; q += 3) {
      CONV_LD(2, q + 2, true); SB(); CONV_MM(0, true); SB();
      CONV_LD(0, q + 3, true); SB(); CONV_MM(1, true); SB();
      if (q + 4 < 32) { CONV_LD(1, q + 4, true); } else { CONV_LD(1, q + 4, false); }
      SB(); CONV_MM(2, true); SB();
    }
    CONV_LD(2, 32, false); SB(); CONV_MM(0, true); SB();
    CONV_LD(0, 33, false); SB(); CONV_MM(1, true); SB();
    for (int q = 32; q < 62; q += 3) {
      CONV_LD(1, q + 2, false); SB(); CONV_MM(2, false); SB();
      CONV_LD(2, q + 3, false); SB(); CONV_MM(0, false); SB();
      if (q + 4 < 64) { CONV_LD(0, q + 4, false); }
      SB(); CONV_MM(1, false); SB();
    }
    CONV_MM(2, false); SB();
    CONV_MM(0, false); SB();
    STAMP(3, 2);
    char* yb = smem + wave * HC_CH;
#pragma unroll
    for (int q4 = 0; q4 < 4; ++q4) {
      u32x2 o0 = {pack_bf16(acc0[4 * q4], acc0[4 * q4 + 1]), pack_bf16(acc0[4 * q4 + 2], acc0[4 * q4 + 3])};
      u32x2 o1 = {pack_bf16(acc1[4 * q4], acc1[4 * q4 + 1]), pack_bf16(acc1[4 * q4 + 2], acc1[4 * q4 + 3])};
      *(u32x2*)(yb + (32 * i + 8 * q4 + 4 * hh) * 2) = o0;
      *(u32x2*)(yb + (32 * (32 + i) + 8 * q4 + 4 * hh) * 2) = o1;
    }
  }
  __syncthreads();
  {
    u32x2 dd = *(const u32x2*)(ssmD + d0);
    float yv[4][2];
    int cidx = 255 - (tid >> 2);
    int off = ((cidx & 3) * UB_S + (cidx >> 2)) * 16 + (3 - (tid & 3)) * 4;
#pragma unroll
    for (int ch = 0; ch < 4; ++ch) {
      unsigned c = *(const unsigned*)(smem + ch * HC_CH + tid * 4);
      unsigned kvp = *(const unsigned*)(ubase + ch * UB_CH + off);
      float dv = chsel(dd, ch);
      yv[ch][0] = rbf(bflo(c) + rbf(bfhi(kvp) * dv));
      yv[ch][1] = rbf(bfhi(c) + rbf(bflo(kvp) * dv));
    }
    char* yo = ws + OFF_Y + ((size_t)cg * 2048 + s0) * 8;
    u32x4 o = {pack_bf16(yv[0][0], yv[1][0]), pack_bf16(yv[2][0], yv[3][0]), pack_bf16(yv[0][1], yv[1][1]),
               pack_bf16(yv[2][1], yv[3][1])};
    *(u32x4*)yo = o;
  }
  STAMP(3, 3);
}

DEVI void layernorm8(const float (&x)[8], float (&o)[8], float2* red, int wave, int t8) {
  float m = 0.f;
#pragma unroll
  for (int j = 0; j < 8; ++j) m += x[j];
  m *= 0.125f;
  float M2 = 0.f;
#pragma unroll
  for (int j = 0; j < 8; ++j) {
    float d = x[j] - m;
    M2 += d * d;
  }
  float n2 = 4.f;
#pragma unroll
  for (int sh = 8; sh <= 32; sh <<= 1) {
    float mo = __shfl_xor(m, sh), M2o = __shfl_xor(M2, sh);
    float d = mo - m;
    M2 = M2 + M2o + d * d * n2;
    m = 0.5f * (m + mo);
    n2 *= 2.f;
  }
  if ((threadIdx.x & 63) < 8) red[wave * 8 + t8] = make_float2(m, M2);
  __syncthreads();
  float2 pr[9];
  float mean = 0.f;
#pragma unroll
  for (int w = 0; w < 9; ++w) {
    pr[w] = red[w * 8 + t8];
    mean += pr[w].x;
  }
  mean *= (1.0f / 9.0f);
  float tq = 0.f;
#pragma unroll
  for (int w = 0; w < 9; ++w) {
    float d = pr[w].x - mean;
    tq += pr[w].y + 64.f * d * d;
  }
  float var = rbf(tq * (1.0f / 576.0f));
  float mb = rbf(mean);
  float sd = rbf(__builtin_amdgcn_sqrtf(rbf(var + 1e-5f)));
  float rs = __builtin_amdgcn_rcpf(sd);
#pragma unroll
  for (int j = 0; j < 8; ++j) o[j] = rbf(rbf(x[j] - mb) * rs);
}

template <int MODE>
__global__ __launch_bounds__(576) void k_row(const u16* __restrict__ q, char* __restrict__ ws, u16* __restrict__ out) {
  __shared__ float2 red[2][72];
  const int tid = threadIdx.x, lane = tid & 63, wave = tid >> 6;
  const int T = blockIdx.x >> 1, half = blockIdx.x & 1;
  const int kp = lane >> 5, G = (lane >> 3) & 3, t8 = lane & 7;
  const int tok = half * 8 + t8, token = T * 16 + tok;
  const int kt = 2 * wave + kp;
  const int chunk = G * 16 + tok;
  const size_t fragoff = ((size_t)(T * 18 + kt) * 64 + chunk) * 16;

  float x[8];
  if (MODE == 0) {
    u32x4 c = *(const u32x4*)(q + (size_t)token * 576 + kt * 32 + G * 8);
    x[0] = bflo(c.x); x[1] = bfhi(c.x); x[2] = bflo(c.y); x[3] = bfhi(c.y);
    x[4] = bflo(c.z); x[5] = bfhi(c.z); x[6] = bflo(c.w); x[7] = bfhi(c.w);
  } else {
    f32x4 pa = {0.f, 0.f, 0.f, 0.f}, pb = {0.f, 0.f, 0.f, 0.f};
#pragma unroll
    for (int sp = 0; sp < 4; ++sp) {
      const char* pp = ws + OFF_P + ((size_t)((sp * 128 + T) * 36 + 2 * kt) * 64 + chunk) * 16;
      pa += *(const f32x4*)pp;
      pb += *(const f32x4*)(pp + 1024);
    }
    u32x4 c = *(const u32x4*)(ws + (MODE == 1 ? OFF_X1 : OFF_X3) + fragoff);
    float xp[8] = {bflo(c.x), bfhi(c.x), bflo(c.y), bfhi(c.y), bflo(c.z), bfhi(c.z), bflo(c.w), bfhi(c.w)};
#pragma unroll
    for (int j = 0; j < 4; ++j) {
      x[j] = rbf(xp[j] + rbf(pa[j]));
      x[4 + j] = rbf(xp[4 + j] + rbf(pb[j]));
    }
  }
  if (MODE == 2) {
    u32x4 o = {pack_bf16(x[0], x[1]), pack_bf16(x[2], x[3]), pack_bf16(x[4], x[5]), pack_bf16(x[6], x[7])};
    *(u32x4*)(out + (size_t)token * 576 + kt * 32 + G * 8) = o;
    return;
  }
  u32x2 ya = *(const u32x2*)(ws + OFF_Y + ((size_t)(8 * kt + 2 * G) * 2048 + token) * 8);
  u32x2 yb = *(const u32x2*)(ws + OFF_Y + ((size_t)(8 * kt + 2 * G + 1) * 2048 + token) * 8);
  float yv[8] = {bflo(ya.x), bfhi(ya.x), bflo(ya.y), bfhi(ya.y), bflo(yb.x), bfhi(yb.x), bflo(yb.y), bfhi(yb.y)};
  float a[8];
  layernorm8(x, a, red[0], wave, t8);
  float xn[8];
#pragma unroll
  for (int j = 0; j < 8; ++j) xn[j] = rbf(x[j] + rbf(a[j] * yv[j]));
  {
    u32x4 o = {pack_bf16(xn[0], xn[1]), pack_bf16(xn[2], xn[3]), pack_bf16(xn[4], xn[5]), pack_bf16(xn[6], xn[7])};
    *(u32x4*)(ws + (MODE == 0 ? OFF_X1 : OFF_X3) + fragoff) = o;
  }
  float b[8];
  layernorm8(xn, b, red[1], wave, t8);
  {
    u32x4 o = {pack_bf16(b[0], b[1]), pack_bf16(b[2], b[3]), pack_bf16(b[4], b[5]), pack_bf16(b[6], b[7])};
    size_t off = ((size_t)(((T >> 4) * 18 + kt) * 16 + (T & 15)) * 64 + chunk) * 16;
    *(u32x4*)(ws + OFF_XN + off) = o;
  }
}

constexpr int STAGE_B = 34 * 1024;
constexpr int NSLOT = 4;
constexpr int GEMM_LDS = NSLOT * STAGE_B;

struct G1State {
  f32x4 acc[9][4];
  bf16x8 xf[2][4];
  bf16x8 wf[9];
};

template <int PIECE>
DEVI void g1_piece(const char* px, const char* pw, unsigned l, int tid, int wave) {
  if (PIECE == 0) aglds16(px + tid * 16, l + wave * 1024);
  if (PIECE == 1) aglds16(px + 8192 + tid * 16, l + 8192 + wave * 1024);
  if (PIECE == 2) aglds16(pw + tid * 16, l + 16384 + wave * 1024);
  if (PIECE == 3) aglds16(pw + 8192 + tid * 16, l + 24576 + wave * 1024);
  if (PIECE == 4) aglds4(pw + 16384 + tid * 4, l + 32768 + wave * 256);
}
#define G1_MM(f) _Pragma("unroll") for (int t = 0; t < 4; ++t) \
    st.acc[f][t] = __builtin_amdgcn_mfma_f32_16x16x32_bf16(st.wf[f], st.xf[S & 1][t], st.acc[f][t], 0, 0, 0)
template <int S>
DEVI void g1_step(G1State& st, const char* px, const char* pw, char* smem, unsigned lbase, int tid, int wave,
                  const char* lx, const char* lw) {
  constexpr int NS = 18;
  if (S + 1 < NS) {
    wait_vm<(S + 2 < NS) ? 5 : 0>();
    bar();
  }
  SB();
  const char* cw = lw + (S % NSLOT) * STAGE_B;
  const char* gx = px + (S + 3) * 16384;
  const char* gw = pw + (S + 3) * 18432;
  const unsigned l = lbase + ((S + 3) % NSLOT) * STAGE_B;
  constexpr bool ISS = (S + 3 < NS);
  G1_MM(0); st.wf[5] = *(const bf16x8*)(cw + 5 * 1024); if (ISS) g1_piece<0>(gx, gw, l, tid, wave); SB();
  G1_MM(1); st.wf[6] = *(const bf16x8*)(cw + 6 * 1024); if (ISS) g1_piece<1>(gx, gw, l, tid, wave); SB();
  G1_MM(2); st.wf[7] = *(const bf16x8*)(cw + 7 * 1024); if (ISS) g1_piece<2>(gx, gw, l, tid, wave); SB();
  G1_MM(3); st.wf[8] = *(const bf16x8*)(cw + 8 * 1024); if (ISS) g1_piece<3>(gx, gw, l, tid, wave); SB();
  G1_MM(4); if (ISS) g1_piece<4>(gx, gw, l, tid, wave); SB();
  const char* nx = lx + ((S + 1) % NSLOT) * STAGE_B;
  const char* nw = lw + ((S + 1) % NSLOT) * STAGE_B;
  constexpr bool NXT = (S + 1 < NS);
  G1_MM(5);
  if (NXT) {
    st.xf[(S + 1) & 1][0] = *(const bf16x8*)(nx);
    st.xf[(S + 1) & 1][1] = *(const bf16x8*)(nx + 1024);
    st.wf[0] = *(const bf16x8*)(nw);
  }
  SB();
  G1_MM(6);
  if (NXT) {
    st.xf[(S + 1) & 1][2] = *(const bf16x8*)(nx + 2048);
    st.xf[(S + 1) & 1][3] = *(const bf16x8*)(nx + 3072);
    st.wf[1] = *(const bf16x8*)(nw + 1024);
  }
  SB();
  G1_MM(7);
  if (NXT) {
    st.wf[2] = *(const bf16x8*)(nw + 2048);
    st.wf[3] = *(const bf16x8*)(nw + 3072);
    st.wf[4] = *(const bf16x8*)(nw + 4096);
  }
  SB();
  G1_MM(8);
  SB();
  if constexpr (S + 1 < NS) g1_step<S + 1>(st, px, pw, smem, lbase, tid, wave, lx, lw);
}

DEVI float silu_f(float g) { return g * __builtin_amdgcn_rcpf(1.0f + __builtin_amdgcn_exp2f(g * -1.44269504088896f)); }

typedef __attribute__((ext_vector_type(3))) unsigned int u32x3;
template <int BM4>
DEVI void g1_epilogue(const G1State& st, char* hb) {
#pragma unroll
  for (int t = 0; t < 4; ++t) {
    unsigned d[9];
#pragma unroll
    for (int f = 0; f < 9; ++f) {
      f32x2 g = {st.acc[f][t][0], st.acc[f][t][1]};
      f32x2 u = {st.acc[f][t][2], st.acc[f][t][3]};
      f32x2 e = g * -1.44269504088896f;
      e[0] = __builtin_amdgcn_exp2f(e[0]);
      e[1] = __builtin_amdgcn_exp2f(e[1]);
      e = e + 1.0f;
      e[0] = __builtin_amdgcn_rcpf(e[0]);
      e[1] = __builtin_amdgcn_rcpf(e[1]);
      f32x2 h = (g * e) * u;
      d[f] = pack_bf16(h[0], h[1]);
    }
    char* p = hb + (size_t)t * 144 * 1024;
    if (BM4 == 0) {
      *(u32x4*)(p) = u32x4{d[0], d[1], d[2], d[3]};
      *(u32x4*)(p + 1024) = u32x4{d[4], d[5], d[6], d[7]};
      *(unsigned*)(p + 2048) = d[8];
    } else if (BM4 == 1) {
      *(u32x3*)(p + 4) = u32x3{d[0], d[1], d[2]};
      *(u32x4*)(p + 1024) = u32x4{d[3], d[4], d[5], d[6]};
      *(u32x2*)(p + 2048) = u32x2{d[7], d[8]};
    } else if (BM4 == 2) {
      *(u32x2*)(p + 8) = u32x2{d[0], d[1]};
      *(u32x4*)(p + 1024) = u32x4{d[2], d[3], d[4], d[5]};
      *(u32x3*)(p + 2048) = u32x3{d[6], d[7], d[8]};
    } else {
      *(unsigned*)(p + 12) = d[0];
      *(u32x4*)(p + 1024) = u32x4{d[1], d[2], d[3], d[4]};
      *(u32x4*)(p + 2048) = u32x4{d[5], d[6], d[7], d[8]};
    }
  }
}

__global__ __launch_bounds__(512, 2) void k_gemm1(char* __restrict__ ws) {
  extern __shared__ __attribute__((aligned(16))) char smem[];
  const int tid = threadIdx.x, lane = tid & 63;
  const int wave = __builtin_amdgcn_readfirstlane(tid >> 6);
  const int bid = blockIdx.x, xcd = bid & 7, idx = bid >> 3;
  const int mb = (xcd & 1) * 4 + (idx & 3), nb = (xcd >> 1) * 8 + (idx >> 2);
  const int wm = wave & 3, wn = wave >> 2;
  const char* px = ws + OFF_XN + (size_t)mb * 18 * 16384;
  const char* pw = ws + OFF_W13 + (size_t)nb * 18 * 18432;
  const char* lx = smem + wm * 4096 + lane * 16;
  const char* lw = smem + 16384 + wn * 9216 + lane * 16;

  STAMP(1, 0);
  G1State st;
#pragma unroll
  for (int f = 0; f < 9; ++f)
#pragma unroll
    for (int t = 0; t < 4; ++t) st.acc[f][t] = f32x4{0.f, 0.f, 0.f, 0.f};

  const unsigned lbase = lds_addr(smem);
#pragma unroll
  for (int s3 = 0; s3 < 3; ++s3) {
    g1_piece<0>(px + s3 * 16384, pw + s3 * 18432, lbase + s3 * STAGE_B, tid, wave);
    g1_piece<1>(px + s3 * 16384, pw + s3 * 18432, lbase + s3 * STAGE_B, tid, wave);
    g1_piece<2>(px + s3 * 16384, pw + s3 * 18432, lbase + s3 * STAGE_B, tid, wave);
    g1_piece<3>(px + s3 * 16384, pw + s3 * 18432, lbase + s3 * STAGE_B, tid, wave);
    g1_piece<4>(px + s3 * 16384, pw + s3 * 18432, lbase + s3 * STAGE_B, tid, wave);
  }
  wait_vm<10>();
  bar();
  STAMP(1, 1);
#pragma unroll
  for (int t = 0; t < 4; ++t) st.xf[0][t] = *(const bf16x8*)(lx + t * 1024);
#pragma unroll
  for (int f = 0; f < 5; ++f) st.wf[f] = *(const bf16x8*)(lw + f * 1024);
  g1_step<0>(st, px, pw, smem, lbase, tid, wave, lx, lw);

  STAMP(1, 2);
  char* hout = ws + OFF_H;
  {
    const int base = nb * 18 + wn * 9;
    char* hb = hout + ((size_t)((mb * 16 + wm * 4) * 144 + (base >> 2)) * 64 + lane) * 16;
    switch (base & 3) {
      case 0: g1_epilogue<0>(st, hb); break;
      case 1: g1_epilogue<1>(st, hb); break;
      case 2: g1_epilogue<2>(st, hb); break;
      default: g1_epilogue<3>(st, hb); break;
    }
  }
  STAMP(1, 3);
}

constexpr int STAGE2_B = 18 * 1024;
struct G2State {
  f32x4 acc[9][2];
  bf16x8 hf[4][2];
  bf16x8 wf[2][9];
};

DEVI void gload16(bf16x8& d, const char* p) {
  asm volatile("global_load_dwordx4 %0, %1, off" : "=v"(d) : "v"(p) : "memory");
}

template <int R, int PIECE>
DEVI void g2_piece(G2State& st, const char* ph, const char* pw, unsigned lbase, int tid, int wave) {
  const unsigned l = lbase + R * STAGE2_B;
  if (PIECE == 0) aglds16(pw + tid * 16, l + wave * 1024);
  if (PIECE == 1) aglds16(pw + 8192 + tid * 16, l + 8192 + wave * 1024);
  if (PIECE == 2) aglds4(pw + 16384 + tid * 4, l + 16384 + wave * 256);
  if (PIECE == 3) gload16(st.hf[R][0], ph);
  if (PIECE == 4) gload16(st.hf[R][1], ph + 144 * 1024);
}
#define G2_MM(f) _Pragma("unroll") for (int t = 0; t < 2; ++t) \
    st.acc[f][t] = __builtin_amdgcn_mfma_f32_16x16x32_bf16(st.wf[S & 1][f], st.hf[S & 3][t], st.acc[f][t], 0, 0, 0)
template <int S>
DEVI void g2_step(G2State& st, const char* ph, const char* pw, unsigned lbase, int tid, int wave, const char* lw) {
  constexpr int NS = 18;
  if (S + 1 < NS) {
    asm volatile("s_waitcnt vmcnt(%2)" : "+v"(st.hf[(S + 1) & 3][0]), "+v"(st.hf[(S + 1) & 3][1]) : "n"((S + 2 < NS) ? 5 : 0) : "memory");
    bar();
  }
  SB();
  constexpr bool NXT = (S + 1 < NS), ISS = (S + 3 < NS);
  constexpr int R = (S + 3) & 3;
  const char* cw = lw + ((S + 1) % NSLOT) * STAGE2_B;
  const char* gh = ph + (S + 3) * 2048;
  const char* gw = pw + (S + 3) * 18432;
  G2_MM(0); if (NXT) { st.wf[(S + 1) & 1][0] = *(const bf16x8*)(cw); st.wf[(S + 1) & 1][1] = *(const bf16x8*)(cw + 1024); } if (ISS) g2_piece<R, 0>(st, gh, gw, lbase, tid, wave); SB();
  G2_MM(1); if (NXT) { st.wf[(S + 1) & 1][2] = *(const bf16x8*)(cw + 2048); st.wf[(S + 1) & 1][3] = *(const bf16x8*)(cw + 3072); } if (ISS) g2_piece<R, 1>(st, gh, gw, lbase, tid, wave); SB();
  G2_MM(2); if (NXT) { st.wf[(S + 1) & 1][4] = *(const bf16x8*)(cw + 4096); st.wf[(S + 1) & 1][5] = *(const bf16x8*)(cw + 5120); } if (ISS) g2_piece<R, 2>(st, gh, gw, lbase, tid, wave); SB();
  G2_MM(3); if (NXT) { st.wf[(S + 1) & 1][6] = *(const bf16x8*)(cw + 6144); st.wf[(S + 1) & 1][7] = *(const bf16x8*)(cw + 7168); } if (ISS) g2_piece<R, 3>(st, gh, gw, lbase, tid, wave); SB();
  G2_MM(4); if (NXT) { st.wf[(S + 1) & 1][8] = *(const bf16x8*)(cw + 8192); } if (ISS) g2_piece<R, 4>(st, gh, gw, lbase, tid, wave); SB();
  G2_MM(5); G2_MM(6); G2_MM(7); G2_MM(8);
  SB();
  if constexpr (S + 1 < NS) g2_step<S + 1>(st, ph, pw, lbase, tid, wave, lw);
}

__global__ __launch_bounds__(512, 2) void k_gemm2(char* __restrict__ ws) {
  extern __shared__ __attribute__((aligned(16))) char smem[];
  const int tid = threadIdx.x, lane = tid & 63;
  const int wave = __builtin_amdgcn_readfirstlane(tid >> 6);
  const int bid = blockIdx.x, xcd = bid & 7, idx = bid >> 3;
  const int sp = xcd >> 1, mt = (xcd & 1) * 8 + (idx & 7), nb2 = idx >> 3;
  const int kg = wave >> 2, wt = wave & 3;
  const char* ph = ws + OFF_H + ((size_t)((mt * 8 + 2 * wt) * 144 + sp * 36 + kg) * 64 + lane) * 16;
  const char* pw = ws + OFF_W2 + (size_t)(nb2 * 144 + sp * 36) * 9216;
  const char* lw = smem + kg * 9216 + lane * 16;

  STAMP(2, 0);
  G2State st;
#pragma unroll
  for (int f = 0; f < 9; ++f)
#pragma unroll
    for (int t = 0; t < 2; ++t) st.acc[f][t] = f32x4{0.f, 0.f, 0.f, 0.f};

  const unsigned lbase = lds_addr(smem);
  g2_piece<0, 0>(st, ph, pw, lbase, tid, wave); g2_piece<0, 1>(st, ph, pw, lbase, tid, wave); g2_piece<0, 2>(st, ph, pw, lbase, tid, wave);
  g2_piece<0, 3>(st, ph, pw, lbase, tid, wave); g2_piece<0, 4>(st, ph, pw, lbase, tid, wave);
  g2_piece<1, 0>(st, ph + 2048, pw + 18432, lbase, tid, wave); g2_piece<1, 1>(st, ph + 2048, pw + 18432, lbase, tid, wave); g2_piece<1, 2>(st, ph + 2048, pw + 18432, lbase, tid, wave);
  g2_piece<1, 3>(st, ph + 2048, pw + 18432, lbase, tid, wave); g2_piece<1, 4>(st, ph + 2048, pw + 18432, lbase, tid, wave);
  g2_piece<2, 0>(st, ph + 4096, pw + 2 * 18432, lbase, tid, wave); g2_piece<2, 1>(st, ph + 4096, pw + 2 * 18432, lbase, tid, wave); g2_piece<2, 2>(st, ph + 4096, pw + 2 * 18432, lbase, tid, wave);
  g2_piece<2, 3>(st, ph + 4096, pw + 2 * 18432, lbase, tid, wave); g2_piece<2, 4>(st, ph + 4096, pw + 2 * 18432, lbase, tid, wave);
  asm volatile("s_waitcnt vmcnt(10)" : "+v"(st.hf[0][0]), "+v"(st.hf[0][1]) :: "memory");
  bar();
  STAMP(2, 1);
#pragma unroll
  for (int f = 0; f < 9; ++f) st.wf[0][f] = *(const bf16x8*)(lw + f * 1024);
  g2_step<0>(st, ph, pw, lbase, tid, wave, lw);
  STAMP(2, 2);
  __syncthreads();
  char* xb = smem + (wt * 18) * 1024 + lane * 16;
  if (kg == 1) {
#pragma unroll
    for (int f = 0; f < 5; ++f)
#pragma unroll
      for (int t = 0; t < 2; ++t) *(f32x4*)(xb + (f * 2 + t) * 1024) = st.acc[f][t];
  } else {
#pragma unroll
    for (int f = 5; f < 9; ++f)
#pragma unroll
      for (int t = 0; t < 2; ++t) *(f32x4*)(xb + (f * 2 + t) * 1024) = st.acc[f][t];
  }
  __syncthreads();
  {
    char* pout = ws + OFF_P;
#pragma unroll
    for (int f = 0; f < 9; ++f) {
      if ((f < 5) == (kg == 0)) {
#pragma unroll
        for (int t = 0; t < 2; ++t) {
          f32x4 o = st.acc[f][t] + *(const f32x4*)(xb + (f * 2 + t) * 1024);
          const int T = mt * 8 + wt * 2 + t;
          *(f32x4*)(pout + ((size_t)((sp * 128 + T) * 36 + nb2 * 9 + f) * 64 + lane) * 16) = o;
        }
      }
    }
  }
  STAMP(2, 3);
}

extern "C" void kernel_launch(void* const* d_in, const int* in_sizes, int n_in, void* d_out, int out_size, void* d_ws,
                              size_t ws_size, hipStream_t stream) {
  const u16* q = (const u16*)d_in[0];
  const u16* kin = (const u16*)d_in[1];
  const u16* vin = (const u16*)d_in[2];
  const u16* shiftk = (const u16*)d_in[3];
  const u16* ssmk = (const u16*)d_in[4];
  const u16* ssmD = (const u16*)d_in[5];
  const u16* w1 = (const u16*)d_in[7];
  const u16* w3 = (const u16*)d_in[8];
  const u16* w2 = (const u16*)d_in[9];
  char* ws = (char*)d_ws;
  u16* out = (u16*)d_out;

  hipFuncSetAttribute((const void*)k_conv, hipFuncAttributeMaxDynamicSharedMemorySize, PREP_LDS);
  hipFuncSetAttribute((const void*)k_gemm1, hipFuncAttributeMaxDynamicSharedMemorySize, GEMM_LDS);
  hipFuncSetAttribute((const void*)k_gemm2, hipFuncAttributeMaxDynamicSharedMemorySize, 4 * STAGE2_B);

  k_conv<<<256, 1024, PREP_LDS, stream>>>(kin, vin, shiftk, ssmk, ssmD, w1, w3, w2, ws);
  k_row<0><<<256, 576, 0, stream>>>(q, ws, out);
  k_gemm1<<<256, 512, GEMM_LDS, stream>>>(ws);
  k_gemm2<<<256, 512, 4 * STAGE2_B, stream>>>(ws);
  k_row<1><<<256, 576, 0, stream>>>(q, ws, out);
  k_gemm1<<<256, 512, GEMM_LDS, stream>>>(ws);
  k_gemm2<<<256, 512, 4 * STAGE2_B, stream>>>(ws);
  k_row<2><<<256, 576, 0, stream>>>(q, ws, out);
}
```
